```python
import torch

def _softmax(x, axis=-1):
    return torch.softmax(x, dim=axis)
def _gelu(x):
    return torch.nn.functional.gelu(x, approximate="tanh")
def _silu(x):
    return torch.nn.functional.silu(x)
def _sigmoid(x):
    return torch.sigmoid(x)
def _layernorm(x, axis=-1, eps=1e-5):
    m = x.mean(dim=axis, keepdim=True)
    v = x.var(dim=axis, keepdim=True, unbiased=False)
    return (x - m) / torch.sqrt(v + eps)
def _scatter_add(tgt, idx, src, axis=0):
    return tgt.clone().index_add_(axis, idx, src)
def _segment_sum(src, seg, n):
    out = torch.zeros((n,) + src.shape[1:], dtype=src.dtype)
    return out.index_add_(0, seg, src)
def _pad(x, pw):
    flat = tuple(v for pair in reversed(pw) for v in pair)
    return torch.nn.functional.pad(x, flat)

kernel_name = 'sequential_preLN__sliding_block_cached__glu_gated_bilinear__decode'

def setup_inputs():

    _w = (lambda a: (lambda t: t.to(torch.bfloat16) if t.is_floating_point() else t)(torch.as_tensor(a)))
    def _A_setup():
        B, Sq, D = 4, 1, 512
        Hq, Hkv, Sk, Dh = 32, 32, 4096, 128
        assert Hq % Hkv == 0, (Hq, Hkv)
        return {
            "x": _w((lambda *s: torch.randn(*s, dtype=torch.bfloat16) * 0.05)(B, Sq, D)),
            "k_cache": _w((lambda *s: torch.randn(*s, dtype=torch.bfloat16) * 1.6)(B, Hkv, Sk, Dh)),
            "v_cache": _w((lambda *s: torch.randn(*s, dtype=torch.bfloat16) * 0.8)(B, Hkv, Sk, Dh)),
            "wq": _w((lambda *s: torch.randn(*s, dtype=torch.bfloat16) * 1.6)(D, Hq * Dh) * (D ** -0.5)),
            "wo": _w((lambda *s: torch.randn(*s, dtype=torch.bfloat16) * 0.8)(Hq * Dh, D) * ((Hq * Dh) ** -0.5)),
        }
    _A_in = _A_setup()

    def _B_setup():
        B, S, D, F = 4, 1, 512, 1376
        return {
            "x": _w((lambda *s: torch.randn(*s, dtype=torch.bfloat16) * 0.05)(B, S, D)),
            "w_gate": _w((lambda *s: torch.randn(*s, dtype=torch.bfloat16) * 0.4)(D, F) * (D ** -0.5)),
            "w_up": _w((lambda *s: torch.randn(*s, dtype=torch.bfloat16) * 0.4)(D, F) * (D ** -0.5)),
            "w_down": _w((lambda *s: torch.randn(*s, dtype=torch.bfloat16) * 0.4)(F, D) * (F ** -0.5)),
        }
    _B_in = _B_setup()

    return {
        "A_x": _A_in["x"],
        "A_k_cache": _A_in["k_cache"],
        "A_v_cache": _A_in["v_cache"],
        "A_wq": _A_in["wq"],
        "A_wo": _A_in["wo"],
        "B_x": _B_in["x"],
        "B_w_gate": _B_in["w_gate"],
        "B_w_up": _B_in["w_up"],
        "B_w_down": _B_in["w_down"],
    }

def reference(A_x, A_k_cache, A_v_cache, A_wq, A_wo, B_x, B_w_gate, B_w_up, B_w_down):
    def _A_ref(x, k_cache, v_cache, wq, wo):
        B, Sq, D = x.shape
        Hq, Dh = 32, 128
        Hkv, Sk = k_cache.shape[1], k_cache.shape[2]
        W = min(4096, max(256, Sk // 4))
        rep = Hq // Hkv
        q = (lambda x, *d: x.permute(*d))((x @ wq).reshape(B, Sq, Hq, Dh), 0, 2, 1, 3)
        k = (lambda x, n, axis: torch.repeat_interleave(x, n, dim=axis))(k_cache[:, :, Sk - W:, :], rep, axis=1)
        v = (lambda x, n, axis: torch.repeat_interleave(x, n, dim=axis))(v_cache[:, :, Sk - W:, :], rep, axis=1)
        scores = torch.einsum("bhqd,bhkd->bhqk", q, k) * (Dh ** -0.5)
        p = _softmax(scores, axis=-1)
        o = torch.einsum("bhqk,bhkd->bhqd", p, v)
        return (lambda x, *d: x.permute(*d))(o, 0, 2, 1, 3).reshape(B, Sq, Hq * Dh) @ wo

    def _B_ref(x, w_gate, w_up, w_down):
        g = torch.einsum("bsd,df->bsf", x, w_gate)
        u = torch.einsum("bsd,df->bsf", x, w_up)
        h = g * u
        return torch.einsum("bsf,fd->bsd", h, w_down)

    x = A_x
    h = x + _A_ref(_layernorm(x, axis=-1), A_k_cache, A_v_cache, A_wq, A_wo)
    h2 = h + _B_ref(_layernorm(h, axis=-1), B_w_gate, B_w_up, B_w_down)
    return h2
```

```cpp
#include <hip/hip_runtime.h>
#include <hip/hip_bf16.h>
#include <stdint.h>

typedef unsigned short u16;
typedef unsigned v4u __attribute__((ext_vector_type(4)));

constexpr int B = 4, D = 512, H = 32, SK = 4096, DH = 128, W = 1024, F = 1376;

__device__ __forceinline__ float bf2f(u16 b) { return __builtin_bit_cast(float, (unsigned)b << 16); }
__device__ __forceinline__ float lo(unsigned u) { return __builtin_bit_cast(float, u << 16); }
__device__ __forceinline__ float hi(unsigned u) { return __builtin_bit_cast(float, u & 0xFFFF0000u); }
__device__ __forceinline__ u16 f2bf(float f) {
  unsigned u = __builtin_bit_cast(unsigned, f);
  u += 0x7FFFu + ((u >> 16) & 1u);
  return (u16)(u >> 16);
}
__device__ __forceinline__ float rbf(float f) { return bf2f(f2bf(f)); }
__device__ __forceinline__ float wsum(float v) {
  for (int o = 32; o; o >>= 1) v += __shfl_xor(v, o);
  return v;
}
__device__ __forceinline__ float wmax(float v) {
  for (int o = 32; o; o >>= 1) v = fmaxf(v, __shfl_xor(v, o));
  return v;
}
__device__ __forceinline__ float bsum(float v, float* red) {
  v = wsum(v);
  __syncthreads();
  if ((threadIdx.x & 63) == 0) red[threadIdx.x >> 6] = v;
  __syncthreads();
  float s = 0;
  for (int i = 0; i < (int)(blockDim.x >> 6); ++i) s += red[i];
  return s;
}
__device__ __forceinline__ float bmax(float v, float* red) {
  v = wmax(v);
  __syncthreads();
  if ((threadIdx.x & 63) == 0) red[threadIdx.x >> 6] = v;
  __syncthreads();
  float s = red[0];
  for (int i = 1; i < (int)(blockDim.x >> 6); ++i) s = fmaxf(s, red[i]);
  return s;
}

__global__ void __launch_bounds__(512) k_attn(const u16* x, const u16* kc, const u16* vc, const u16* wq, float* ws_o, float* ws_ml) {
  __shared__ float xn[D], red[8], q[DH], part[8 * 4][DH], sc[512];
  const int blk = blockIdx.x, h = (blk & 7) + 8 * ((blk >> 3) & 3), r = blk >> 5, b = r >> 1, half = r & 1;
  const int tid = threadIdx.x, lane = tid & 63, w = tid >> 6;
  float xv = bf2f(x[b * D + tid]);
  float mean = bsum(xv, red) * (1.f / D);
  float d = xv - mean;
  float var = bsum(d * d, red) * (1.f / D);
  xn[tid] = rbf(d / sqrtf(var + 1e-5f));
  __syncthreads();
  const int g = lane >> 4, c = lane & 15;
  {
    float acc[8] = {0, 0, 0, 0, 0, 0, 0, 0};
#pragma unroll
    for (int i = 0; i < 16; ++i) {
      int row = 64 * w + 4 * i + g;
      v4u v = *(const v4u*)(wq + (size_t)row * (H * DH) + h * DH + 8 * c);
      float s = xn[row];
#pragma unroll
      for (int j = 0; j < 4; ++j) { acc[2 * j] += s * lo(v[j]); acc[2 * j + 1] += s * hi(v[j]); }
    }
#pragma unroll
    for (int j = 0; j < 8; ++j) part[w * 4 + g][8 * c + j] = acc[j];
  }
  __syncthreads();
  if (tid < DH) { float s = 0; for (int i = 0; i < 32; ++i) s += part[i][tid]; q[tid] = rbf(s); }
  __syncthreads();
  const size_t base = ((size_t)(b * H + h) * SK + (SK - W) + half * 512) * DH;
  float qv[8];
#pragma unroll
  for (int j = 0; j < 8; ++j) qv[j] = q[8 * c + j];
#pragma unroll
  for (int i = 0; i < 16; ++i) {
    int key = 64 * w + 4 * i + g;
    v4u v = *(const v4u*)(kc + base + (size_t)key * DH + 8 * c);
    float s = 0;
#pragma unroll
    for (int j = 0; j < 4; ++j) { s += qv[2 * j] * lo(v[j]); s += qv[2 * j + 1] * hi(v[j]); }
    s += __shfl_xor(s, 1); s += __shfl_xor(s, 2); s += __shfl_xor(s, 4); s += __shfl_xor(s, 8);
    if (c == 0) sc[key] = s * 0.08838834764831845f;
  }
  __syncthreads();
  float s = sc[tid];
  float m = bmax(s, red);
  float p = __expf(s - m);
  float l = bsum(p, red);
  sc[tid] = p;
  __syncthreads();
  {
    float acc[8] = {0, 0, 0, 0, 0, 0, 0, 0};
#pragma unroll
    for (int i = 0; i < 16; ++i) {
      int key = 64 * w + 4 * i + g;
      v4u v = *(const v4u*)(vc + base + (size_t)key * DH + 8 * c);
      float pp = sc[key];
#pragma unroll
      for (int j = 0; j < 4; ++j) { acc[2 * j] += pp * lo(v[j]); acc[2 * j + 1] += pp * hi(v[j]); }
    }
#pragma unroll
    for (int j = 0; j < 8; ++j) part[w * 4 + g][8 * c + j] = acc[j];
  }
  __syncthreads();
  const int slot = (b * H + h) * 2 + half;
  if (tid < DH) { float t = 0; for (int i = 0; i < 32; ++i) t += part[i][tid]; ws_o[slot * DH + tid] = t; }
  if (tid == 0) { ws_ml[slot * 2] = m; ws_ml[slot * 2 + 1] = l; }
}

__global__ void __launch_bounds__(512) k_wo(const u16* wo, const float* ws_o, const float* ws_ml, float* ws_y) {
  __shared__ float o[B][DH];
  __shared__ float part[8][B][D];
  const int h = blockIdx.x, tid = threadIdx.x, lane = tid & 63, w = tid >> 6;
  {
    int b = tid >> 7, dd = tid & 127;
    int s0 = (b * H + h) * 2;
    float m0 = ws_ml[s0 * 2], l0 = ws_ml[s0 * 2 + 1], m1 = ws_ml[s0 * 2 + 2], l1 = ws_ml[s0 * 2 + 3];
    float m = fmaxf(m0, m1), a0 = __expf(m0 - m), a1 = __expf(m1 - m);
    o[b][dd] = rbf((ws_o[s0 * DH + dd] * a0 + ws_o[(s0 + 1) * DH + dd] * a1) / (l0 * a0 + l1 * a1));
  }
  __syncthreads();
  float acc[B][8] = {};
#pragma unroll
  for (int i = 0; i < 16; ++i) {
    int row = 16 * w + i;
    v4u v = *(const v4u*)(wo + (size_t)(h * DH + row) * D + 8 * lane);
#pragma unroll
    for (int b = 0; b < B; ++b) {
      float s = o[b][row];
#pragma unroll
      for (int j = 0; j < 4; ++j) { acc[b][2 * j] += s * lo(v[j]); acc[b][2 * j + 1] += s * hi(v[j]); }
    }
  }
#pragma unroll
  for (int b = 0; b < B; ++b)
#pragma unroll
    for (int j = 0; j < 8; ++j) part[w][b][8 * lane + j] = acc[b][j];
  __syncthreads();
#pragma unroll
  for (int b = 0; b < B; ++b) {
    float s = 0;
    for (int i = 0; i < 8; ++i) s += part[i][b][tid];
    ws_y[(h * B + b) * D + tid] = s;
  }
}

__global__ void __launch_bounds__(512) k_ffn1(const u16* x, const u16* wg, const u16* wu, const float* ws_y, float* ws_h, float* ws_hh) {
  __shared__ float hn[B][D], red[8];
  __shared__ float part[2][B][32][16 + 1];
  const int j0 = blockIdx.x * 16, tid = threadIdx.x;
  for (int b = 0; b < B; ++b) {
    float s = 0;
    for (int h = 0; h < H; ++h) s += ws_y[(h * B + b) * D + tid];
    float hv = rbf(bf2f(x[b * D + tid]) + rbf(s));
    if (blockIdx.x == 0) ws_h[b * D + tid] = hv;
    float mean = bsum(hv, red) * (1.f / D);
    float d = hv - mean;
    float var = bsum(d * d, red) * (1.f / D);
    hn[b][tid] = rbf(d / sqrtf(var + 1e-5f));
  }
  __syncthreads();
  const int c = tid & 15, g = tid >> 4;
  float ag[B] = {}, au[B] = {};
  for (int i = 0; i < 16; ++i) {
    int row = g * 16 + i;
    float a = bf2f(wg[(size_t)row * F + j0 + c]), u = bf2f(wu[(size_t)row * F + j0 + c]);
#pragma unroll
    for (int b = 0; b < B; ++b) { ag[b] += hn[b][row] * a; au[b] += hn[b][row] * u; }
  }
#pragma unroll
  for (int b = 0; b < B; ++b) { part[0][b][g][c] = ag[b]; part[1][b][g][c] = au[b]; }
  __syncthreads();
  if (tid < 64) {
    int b = tid >> 4;
    float gg = 0, uu = 0;
    for (int i = 0; i < 32; ++i) { gg += part[0][b][i][c]; uu += part[1][b][i][c]; }
    ws_hh[b * F + j0 + c] = rbf(rbf(gg) * rbf(uu));
  }
}

__global__ void __launch_bounds__(512) k_ffn2(const u16* wd, const float* ws_h, const float* ws_hh, u16* out) {
  __shared__ float hh[B][F];
  __shared__ float part[B][32][17];
  const int j0 = blockIdx.x * 16, tid = threadIdx.x;
  for (int i = tid; i < B * F; i += 512) hh[i / F][i % F] = ws_hh[i];
  __syncthreads();
  const int c = tid & 15, g = tid >> 4;
  float a[B] = {};
  for (int row = g; row < F; row += 32) {
    float wv = bf2f(wd[(size_t)row * D + j0 + c]);
#pragma unroll
    for (int b = 0; b < B; ++b) a[b] += hh[b][row] * wv;
  }
#pragma unroll
  for (int b = 0; b < B; ++b) part[b][g][c] = a[b];
  __syncthreads();
  if (tid < 64) {
    int b = tid >> 4;
    float s = 0;
    for (int i = 0; i < 32; ++i) s += part[b][i][c];
    out[b * D + j0 + c] = f2bf(ws_h[b * D + j0 + c] + rbf(s));
  }
}

extern "C" void kernel_launch(void* const* d_in, const int* in_sizes, int n_in, void* d_out, int out_size, void* d_ws,
                              size_t ws_size, hipStream_t stream) {
  const u16* x = (const u16*)d_in[0];
  const u16* kc = (const u16*)d_in[1];
  const u16* vc = (const u16*)d_in[2];
  const u16* wq = (const u16*)d_in[3];
  const u16* wo = (const u16*)d_in[4];
  const u16* wg = (const u16*)d_in[6];
  const u16* wu = (const u16*)d_in[7];
  const u16* wd = (const u16*)d_in[8];
  float* ws = (float*)d_ws;
  float* ws_o = ws;
  float* ws_ml = ws_o + 256 * DH;
  float* ws_y = ws_ml + 512;
  float* ws_h = ws_y + H * B * D;
  float* ws_hh = ws_h + B * D;
  k_attn<<<256, 512, 0, stream>>>(x, kc, vc, wq, ws_o, ws_ml);
  k_wo<<<32, 512, 0, stream>>>(wo, ws_o, ws_ml, ws_y);
  k_ffn1<<<86, 512, 0, stream>>>(x, wg, wu, ws_y, ws_h, ws_hh);
  k_ffn2<<<32, 512, 0, stream>>>(wd, ws_h, ws_hh, (u16*)d_out);
}
```

```cpp
#include <hip/hip_runtime.h>
#include <stdint.h>

typedef unsigned short u16;
typedef unsigned u32;
typedef u32 v4u __attribute__((ext_vector_type(4)));
typedef u32 v2u __attribute__((ext_vector_type(2)));
typedef float v4f __attribute__((ext_vector_type(4)));
typedef float v2f __attribute__((ext_vector_type(2)));
typedef __bf16 bf2 __attribute__((ext_vector_type(2)));
typedef __bf16 bf8 __attribute__((ext_vector_type(8)));
typedef __amdgpu_buffer_rsrc_t rsrc_t;
#define DI __device__ __forceinline__
#define SB() __builtin_amdgcn_sched_barrier(0)

constexpr int D = 512, NH = 32, SK = 4096, DH = 128, WIN = 1024, F = 1376;
constexpr int NJ = F / 32;
constexpr u32 OFF_ML = 4096;
constexpr u32 OFF_H = 8192;
constexpr u32 OFF_Q = 16384;
constexpr u32 OFF_Y = OFF_Q + (1u << 20);
constexpr u32 OFF_P = OFF_Y + (1u << 19);
constexpr u32 WS_BYTES = OFF_P + (1u << 19);
constexpr int NT = 2, SC1 = 16;
constexpr int SPIN = 400000;
constexpr u32 PLO = 0x05040100u, PHI = 0x07060302u;
constexpr float LOG2E = 1.4426950408889634f;

#define TS(i)

DI float f_(u32 u) { return __builtin_bit_cast(float, u); }
DI u32 u_(float f) { return __builtin_bit_cast(u32, f); }
DI float blo(u32 u) { return f_(u << 16); }
DI float bhi(u32 u) { return f_(u & 0xffff0000u); }
DI u32 pk(float a, float b) {
  v2f v = {a, b};
  return __builtin_bit_cast(u32, __builtin_convertvector(v, bf2));
}
DI float rbf(float a) { return f_(pk(a, a) & 0xffff0000u); }
DI float dot2(u32 a, u32 b, float c) {
  return __builtin_amdgcn_fdot2_f32_bf16(__builtin_bit_cast(bf2, a), __builtin_bit_cast(bf2, b), c, false);
}
DI u32 prm(u32 second, u32 first, u32 sel) { return __builtin_amdgcn_perm(second, first, sel); }
template <int C> DI u32 dppu(u32 v) { return (u32)__builtin_amdgcn_mov_dpp((int)v, C, 0xf, 0xf, true); }
template <int C> DI float dppf(float v) { return f_(dppu<C>(u_(v))); }
constexpr int X1 = 0xB1, X2 = 0x4E, HM = 0x141, RM = 0x140, R4 = 0x124, R8 = 0x128;
DI float sw32(float a, float b) {
  auto r = __builtin_amdgcn_permlane32_swap(u_(a), u_(b), false, false);
  return f_(r[0]) + f_(r[1]);
}
DI float sw16(float a, float b) {
  auto r = __builtin_amdgcn_permlane16_swap(u_(a), u_(b), false, false);
  return f_(r[0]) + f_(r[1]);
}
DI float mx32(float a) {
  auto r = __builtin_amdgcn_permlane32_swap(u_(a), u_(a), false, false);
  return fmaxf(f_(r[0]), f_(r[1]));
}
DI float mx16(float a) {
  auto r = __builtin_amdgcn_permlane16_swap(u_(a), u_(a), false, false);
  return fmaxf(f_(r[0]), f_(r[1]));
}
DI float rowsum(float v) {
  v += dppf<X1>(v); v += dppf<X2>(v); v += dppf<HM>(v); v += dppf<RM>(v);
  return v;
}
DI float wavesum(float v) { v = rowsum(v); v = sw16(v, v); return sw32(v, v); }
DI float ex2(float v) { return __builtin_amdgcn_exp2f(v); }
DI float rcp(float v) { return __builtin_amdgcn_rcpf(v); }

DI rsrc_t mkr(const void* p, u32 bytes) { return __builtin_amdgcn_make_buffer_rsrc((void*)p, 0, bytes, 0x00020000); }
template <int AUX> DI v4u ld(rsrc_t r, u32 voff, u32 soff) { return __builtin_amdgcn_raw_buffer_load_b128(r, voff, soff, AUX); }
DI void pub(rsrc_t r, u32 off, u32 a, u32 t0, u32 b, u32 t1) {
  v4u v = {a, t0, b, t1};
  __builtin_amdgcn_raw_buffer_store_b128(v, r, off, 0, SC1);
}
DI void bar() { asm volatile("s_waitcnt lgkmcnt(0)\n\ts_barrier" ::: "memory"); }

template <int N> DI void poll(v4u (&v)[N], rsrc_t r, u32 voff, u32 stride, u32 t0, u32 t1) {
  for (int it = 0; it < SPIN; ++it) {
    asm volatile("" ::: "memory");
#pragma unroll
    for (int n = 0; n < N; ++n) v[n] = ld<SC1>(r, voff, n * stride);
    bool ok = true;
#pragma unroll
    for (int n = 0; n < N; ++n) ok = ok && (v[n].y == t0) && (v[n].w == t1);
    if (__all(ok)) break;
  }
}

DI float ln_rinv(float var) { return rcp(rbf(__builtin_amdgcn_sqrtf(rbf(rbf(var) + 1e-5f)))); }
DI u32 ln_pair(u32 v, float m, float rinv) { return pk(rbf(blo(v) - m) * rinv, rbf(bhi(v) - m) * rinv); }

DI void colred(const float (&a)[8], u32& p01, u32& p23) {
  float s[4];
#pragma unroll
  for (int k = 0; k < 4; ++k) s[k] = sw32(a[k], a[k + 4]);
#pragma unroll
  for (int k = 0; k < 4; ++k) s[k] = sw16(s[k], s[k]);
#pragma unroll
  for (int k = 0; k < 4; ++k) s[k] += dppf<R8>(s[k]);
  p01 = pk(s[0], s[1]);
  p23 = pk(s[2], s[3]);
}
DI float red4(float v0, float v1, float v2, float v3) {
  float s0 = sw32(v0, v2), s1 = sw32(v1, v3);
  float t = sw16(s0, s1);
  return rowsum(t);
}

__global__ void __launch_bounds__(512, 2)
fused_decode(const u16* __restrict__ x, const u16* __restrict__ kc, const u16* __restrict__ vc, const u16* __restrict__ wq,
             const u16* __restrict__ wo, const u16* __restrict__ wg, const u16* __restrict__ wu,
             const u16* __restrict__ wd, u16* __restrict__ out, unsigned char* __restrict__ ws) {
  __shared__ __attribute__((aligned(16))) u32 qs[64];
  __shared__ __attribute__((aligned(16))) float ml[16];
  __shared__ __attribute__((aligned(16))) float op[8 * 128];
  __shared__ __attribute__((aligned(16))) u32 ob[64];
  __shared__ __attribute__((aligned(16))) u32 hn[256];
  __shared__ __attribute__((aligned(16))) float red[8 * 64];
  TS(0);
  const int blk = blockIdx.x, xc = blk & 7, ii = blk >> 3;
  const int h = 4 * xc + (ii & 3), r = ii >> 2, b = r >> 1, half = r & 1;
  const int tid = threadIdx.x, lane = tid & 63, w = __builtin_amdgcn_readfirstlane(tid >> 6);
  const int g = lane >> 4, c = lane & 15, r8 = lane >> 3, c8 = lane & 7;

  asm volatile("" ::"s"(x), "s"(kc), "s"(vc), "s"(wq), "s"(wo), "s"(wg), "s"(wu), "s"(wd), "s"(out), "s"(ws));
  const rsrc_t rws = mkr(ws, WS_BYTES);
  v2u ep = __builtin_amdgcn_raw_buffer_load_b64(rws, 0, 0, SC1);
  u32 ta = 0, tb = 0;
  const size_t kvbase = ((size_t)(b * NH + h) * SK + (SK - WIN) + half * 512) * DH;
  const rsrc_t rk = mkr(kc + kvbase, 512 * DH * 2);
  const rsrc_t rv = mkr(vc + kvbase, 512 * DH * 2);
  const rsrc_t rwo = mkr(wo + (size_t)h * DH * D, DH * D * 2);
  const rsrc_t rx = mkr(x, 4 * D * 2);
  const u32 kvoff = (64 * w + g) * 256 + 16 * c;
  const u32 wooff = 16 * r8 * 1024 + 128 * w + 16 * c8;

  if (w == 0) {
    const rsrc_t rq = mkr(wq + (size_t)64 * r * (NH * DH) + h * DH, 64 * NH * DH * 2 - h * DH * 2);
    const int bb = c >> 2, sub = (c & 3) + 4 * g;
    v4u xs[4], xa[2], q[16];
#pragma unroll
    for (int k = 0; k < 4; ++k) xs[k] = ld<0>(rx, bb * 1024 + 16 * sub, 256 * k);
#pragma unroll
    for (int k = 0; k < 2; ++k) xa[k] = ld<0>(rx, bb * 1024 + 128 * r + 32 * g, 16 * k);
    SB();
#pragma unroll
    for (int i = 0; i < 16; ++i) q[i] = ld<NT>(rq, 16 * g * 8192 + 16 * c, i * 8192);
    SB();
    float s = 0.f;
#pragma unroll
    for (int k = 0; k < 4; ++k)
#pragma unroll
      for (int e = 0; e < 4; ++e) s += blo(xs[k][e]) + bhi(xs[k][e]);
    s += dppf<X1>(s); s += dppf<X2>(s);
    s = sw16(s, s); s = sw32(s, s);
    const float mean = s * (1.f / D);
    float vs = 0.f;
#pragma unroll
    for (int k = 0; k < 4; ++k)
#pragma unroll
      for (int e = 0; e < 4; ++e) {
        float d0 = blo(xs[k][e]) - mean, d1 = bhi(xs[k][e]) - mean;
        vs = fmaf(d0, d0, vs); vs = fmaf(d1, d1, vs);
      }
    vs += dppf<X1>(vs); vs += dppf<X2>(vs);
    vs = sw16(vs, vs); vs = sw32(vs, vs);
    const float mb = rbf(mean), rinv = ln_rinv(vs * (1.f / D));
    v4u an[2];
#pragma unroll
    for (int k = 0; k < 2; ++k)
#pragma unroll
      for (int e = 0; e < 4; ++e) an[k][e] = ln_pair(xa[k][e], mb, rinv);
    float qa[8];
#pragma unroll
    for (int j = 0; j < 8; ++j) {
      v4f acc = {0.f, 0.f, 0.f, 0.f};
#pragma unroll
      for (int k = 0; k < 2; ++k) {
        v4u bq;
#pragma unroll
        for (int t = 0; t < 4; ++t) bq[t] = prm(q[8 * k + 2 * t + 1][j >> 1], q[8 * k + 2 * t][j >> 1], (j & 1) ? PHI : PLO);
        acc = __builtin_amdgcn_mfma_f32_16x16x32_bf16(__builtin_bit_cast(bf8, an[k]), __builtin_bit_cast(bf8, bq), acc, 0, 0, 0);
      }
      qa[j] = acc[0];
    }
    ta = ep.x; tb = ep.y;
    asm volatile("" : "+v"(ta), "+v"(tb));
    ta += 1u; tb += 1u;
    const u32 qo = OFF_Q + (((h * 4 + g) * 8 + r) * 64 + c) * 16;
#pragma unroll
    for (int t = 0; t < 4; ++t) pub(rws, qo + 256 * t, u_(qa[2 * t]), ta, u_(qa[2 * t + 1]), tb);
    SB();
  }
  TS(1);

  v4u kk[16], vv[16], ww[16];
#pragma unroll
  for (int i = 0; i < 16; ++i) kk[i] = ld<NT>(rk, kvoff, i * 1024);
  SB();
  if (w == 0) {
    v4u qp[8];
    poll<8>(qp, rws, OFF_Q + ((h * 4 + b) * 8 * 64 + lane) * 16, 1024, ta, tb);
    float q0 = 0.f, q1 = 0.f;
#pragma unroll
    for (int n = 0; n < 8; ++n) { q0 += f_(qp[n].x); q1 += f_(qp[n].z); }
    qs[4 * c + g] = pk(q0, q1);
    SB();
  }
#pragma unroll
  for (int i = 0; i < 4; ++i) { vv[i] = ld<NT>(rv, kvoff, i * 1024); ww[i] = ld<NT>(rwo, wooff, i * 1024); }
  SB();
  u32 t0 = ep.x, t1 = ep.y;
  asm volatile("" : "+v"(t0), "+v"(t1));
  t0 += 1u; t1 += 1u;
  bar();
  TS(2);

  float p, mw;
  {
    const v4u qv = *(const v4u*)&qs[4 * c];
    float s[16];
#pragma unroll
    for (int i = 0; i < 16; ++i) {
      float a = dot2(kk[i].x, qv.x, 0.f);
      a = dot2(kk[i].y, qv.y, a);
      a = dot2(kk[i].z, qv.z, a);
      s[i] = dot2(kk[i].w, qv.w, a);
    }
    const bool b3 = c & 8, b2 = c & 4, b1 = c & 2, b0 = c & 1;
#pragma unroll
    for (int i = 0; i < 8; ++i) s[i] = (b3 ? s[i + 8] : s[i]) + dppf<RM>(b3 ? s[i] : s[i + 8]);
#pragma unroll
    for (int i = 0; i < 4; ++i) s[i] = (b2 ? s[i + 4] : s[i]) + dppf<HM>(b2 ? s[i] : s[i + 4]);
#pragma unroll
    for (int i = 0; i < 2; ++i) s[i] = (b1 ? s[i + 2] : s[i]) + dppf<X2>(b1 ? s[i] : s[i + 2]);
    const float raw = (b0 ? s[1] : s[0]) + dppf<X1>(b0 ? s[0] : s[1]);
    const float sc = rbf(rbf(raw) * 0.08838834764831845f);
    float m = sc;
    m = fmaxf(m, dppf<X1>(m)); m = fmaxf(m, dppf<X2>(m)); m = fmaxf(m, dppf<HM>(m)); m = fmaxf(m, dppf<RM>(m));
    m = mx32(mx16(m));
    mw = m;
    p = ex2((sc - m) * LOG2E);
    const float l = wavesum(p);
    if (lane == 0) { v2f t = {m, l}; *(v2f*)&ml[2 * w] = t; }
  }
  bar();
  TS(3);
  float fw;
  {
    const v2f t = *(const v2f*)&ml[2 * c8];
    float M = t.x;
    M = fmaxf(M, dppf<X1>(M)); M = fmaxf(M, dppf<X2>(M)); M = fmaxf(M, dppf<HM>(M));
    float L = t.y * ex2((t.x - M) * LOG2E);
    L += dppf<X1>(L); L += dppf<X2>(L); L += dppf<HM>(L);
    fw = ex2((mw - M) * LOG2E) * rcp(L);
    if (w == 0 && lane == 0) pub(rws, OFF_ML + (b * 64 + h * 2 + half) * 16, u_(M), t0, u_(L), t1);
  }
  const u32 ppk = pk(p, dppf<X1>(p));
  SB();

  float acc[8] = {0.f, 0.f, 0.f, 0.f, 0.f, 0.f, 0.f, 0.f};
  u32 wp[8][8];
#define PVSTEP(t)                                                                       \
  do {                                                                                  \
    const u32 pp = dppu<0x150 + 2 * (t)>(ppk);                                          \
    _Pragma("unroll") for (int e = 0; e < 4; ++e) {                                     \
      acc[2 * e] = dot2(prm(vv[2 * (t) + 1][e], vv[2 * (t)][e], PLO), pp, acc[2 * e]);  \
      acc[2 * e + 1] = dot2(prm(vv[2 * (t) + 1][e], vv[2 * (t)][e], PHI), pp, acc[2 * e + 1]); \
      wp[t][2 * e] = prm(ww[2 * (t) + 1][e], ww[2 * (t)][e], PLO);                      \
      wp[t][2 * e + 1] = prm(ww[2 * (t) + 1][e], ww[2 * (t)][e], PHI);                  \
    }                                                                                   \
    _Pragma("unroll") for (int e = 0; e < 8; ++e) asm volatile("" : "+v"(wp[t][e]));    \
    SB();                                                                               \
  } while (0)
#define VWLD(i) do { vv[i] = ld<NT>(rv, kvoff, (i) * 1024); ww[i] = ld<NT>(rwo, wooff, (i) * 1024); } while (0)
  VWLD(4); VWLD(5); VWLD(6); VWLD(7); SB();
  PVSTEP(0);
  VWLD(8); VWLD(9); SB();
  PVSTEP(1);
  VWLD(10); VWLD(11); SB();
  PVSTEP(2);
  VWLD(12); VWLD(13); SB();
  PVSTEP(3);
  VWLD(14); VWLD(15); SB();
  PVSTEP(4); PVSTEP(5); PVSTEP(6); PVSTEP(7);
  TS(4);
  {
    float s[4];
#pragma unroll
    for (int k = 0; k < 4; ++k) s[k] = sw32(acc[k], acc[k + 4]);
    v2f o = {sw16(s[0], s[2]) * fw, sw16(s[1], s[3]) * fw};
    *(v2f*)&op[w * 128 + 8 * c + 2 * g] = o;
  }
  bar();
  {
    const v2f t = *(const v2f*)&op[r8 * 128 + 16 * w + 2 * c8];
    float a = t.x + dppf<R8>(t.x), d = t.y + dppf<R8>(t.y);
    a = sw16(a, a); d = sw16(d, d);
    a = sw32(a, a); d = sw32(d, d);
    if (lane < 8) ob[8 * w + lane] = pk(a, d);
  }
  bar();
  TS(5);
  {
    const v4u o0 = *(const v4u*)&ob[8 * r8], o1 = *(const v4u*)&ob[8 * r8 + 4];
    float a[8] = {0.f, 0.f, 0.f, 0.f, 0.f, 0.f, 0.f, 0.f};
#pragma unroll
    for (int t = 0; t < 8; ++t)
#pragma unroll
      for (int e = 0; e < 8; ++e) a[e] = dot2(wp[t][e], t < 4 ? o0[t & 3] : o1[t & 3], a[e]);
    u32 p01, p23;
    colred(a, p01, p23);
    const int cg = 16 * w + 2 * c8 + (lane >> 5);
    if ((lane & 24) == 0) pub(rws, OFF_Y + ((b * 128 + cg) * 64 + h * 2 + half) * 16, p01, t0, p23, t1);
  }
  TS(6);

  if (ii >= 24) {
    const int ri = xc + 8 * (ii - 24), rb = ri >> 4, cg = 8 * (ri & 15) + w;
    const u32 xraw = x[rb * D + 4 * cg + g];
    v4u mlv[1], yv[1];
    poll<1>(mlv, rws, OFF_ML + (rb * 64 + lane) * 16, 0, t0, t1);
    const float M = f_(mlv[0].x), L = f_(mlv[0].z);
    const float Mo = dppf<X1>(M), Lo = dppf<X1>(L);
    const float Ms = fmaxf(M, Mo);
    const float e = L * ex2((M - Ms) * LOG2E), eo = Lo * ex2((Mo - Ms) * LOG2E);
    const float wt = e * rcp(e + eo);
    TS(7);
    poll<1>(yv, rws, OFF_Y + ((rb * 128 + cg) * 64 + lane) * 16, 0, t0, t1);
    TS(8);
    const float y = red4(blo(yv[0].x) * wt, bhi(yv[0].x) * wt, blo(yv[0].z) * wt, bhi(yv[0].z) * wt);
    const float hv = rbf(blo(xraw) + rbf(y));
    {
      auto a = __builtin_amdgcn_permlane16_swap(u_(hv), u_(hv), false, false);
      const u32 hp = prm(a[1], a[0], PHI);
      auto d = __builtin_amdgcn_permlane32_swap(hp, hp, false, false);
      if (lane == 0) pub(rws, OFF_H + (rb * 128 + cg) * 16, d[0], t0, d[1], t1);
    }
    TS(9);
    if (ri == 0 && w == 0) {
      v4u hv3[1];
      poll<1>(hv3, rws, OFF_H + ((lane & 3) * 128) * 16, 0, t0, t1);
    }
    v4u pv[1];
    const int j = lane < NJ ? lane : NJ - 1;
    poll<1>(pv, rws, OFF_P + ((rb * 128 + cg) * 64 + j) * 16, 0, t0, t1);
    TS(10);
    const bool on = lane < NJ;
    const float dn = red4(on ? blo(pv[0].x) : 0.f, on ? bhi(pv[0].x) : 0.f, on ? blo(pv[0].z) : 0.f, on ? bhi(pv[0].z) : 0.f);
    const u32 o = pk(hv + rbf(dn), 0.f);
    if (c == 0) out[rb * D + 4 * cg + g] = (u16)o;
    if (ri == 0 && w == 0 && lane == 0) {
      v2u e2 = {t0, t1};
      __builtin_amdgcn_raw_buffer_store_b64(e2, rws, 0, 0, SC1);
    }
    TS(11);
  } else {
    const int j = 2 * (xc + 8 * (ii >> 3)) + ((ii >> 2) & 1), fb = ii & 3;
    if (j < NJ) {
      const int r16 = lane >> 2, c4 = lane & 3;
      const rsrc_t rg = mkr(wg + 32 * j, D * F * 2 - 64 * j);
      const rsrc_t ru = mkr(wu + 32 * j, D * F * 2 - 64 * j);
      const rsrc_t rd = mkr(wd + (size_t)32 * j * D, 32 * D * 2);
      const u32 goff = (64 * w + 4 * r16) * (F * 2) + 16 * c4;
      const u32 doff = 4 * r8 * 1024 + 128 * w + 16 * c8;
      v4u gw[4], uw[4], dw[4];
#pragma unroll
      for (int i = 0; i < 4; ++i) { gw[i] = ld<NT>(rg, goff, i * F * 2); uw[i] = ld<NT>(ru, goff, i * F * 2); }
#pragma unroll
      for (int i = 0; i < 4; ++i) dw[i] = ld<NT>(rd, doff, i * 1024);
      SB();
      if (w == 0) {
        v4u hv[2];
        poll<2>(hv, rws, OFF_H + (fb * 128 + lane) * 16, 1024, t0, t1);
        TS(7);
        float v[8] = {blo(hv[0].x), bhi(hv[0].x), blo(hv[0].z), bhi(hv[0].z), blo(hv[1].x), bhi(hv[1].x), blo(hv[1].z), bhi(hv[1].z)};
        const float mean = wavesum(((v[0] + v[1]) + (v[2] + v[3])) + ((v[4] + v[5]) + (v[6] + v[7]))) * (1.f / D);
        float vs = 0.f;
#pragma unroll
        for (int k = 0; k < 8; ++k) { float d = v[k] - mean; vs = fmaf(d, d, vs); }
        const float mb = rbf(mean), rinv = ln_rinv(wavesum(vs) * (1.f / D));
        v2u a = {ln_pair(hv[0].x, mb, rinv), ln_pair(hv[0].z, mb, rinv)};
        v2u d = {ln_pair(hv[1].x, mb, rinv), ln_pair(hv[1].z, mb, rinv)};
        *(v2u*)&hn[2 * lane] = a;
        *(v2u*)&hn[128 + 2 * lane] = d;
      }
      u32 gp[2][8], up[2][8], dp[2][8];
#pragma unroll
      for (int t = 0; t < 2; ++t)
#pragma unroll
        for (int e = 0; e < 4; ++e) {
          gp[t][2 * e] = prm(gw[2 * t + 1][e], gw[2 * t][e], PLO); gp[t][2 * e + 1] = prm(gw[2 * t + 1][e], gw[2 * t][e], PHI);
          up[t][2 * e] = prm(uw[2 * t + 1][e], uw[2 * t][e], PLO); up[t][2 * e + 1] = prm(uw[2 * t + 1][e], uw[2 * t][e], PHI);
          dp[t][2 * e] = prm(dw[2 * t + 1][e], dw[2 * t][e], PLO); dp[t][2 * e + 1] = prm(dw[2 * t + 1][e], dw[2 * t][e], PHI);
        }
#pragma unroll
      for (int t = 0; t < 2; ++t)
#pragma unroll
        for (int e = 0; e < 8; ++e) asm volatile("" : "+v"(gp[t][e]), "+v"(up[t][e]), "+v"(dp[t][e]));
      bar();
      TS(8);
      {
        const v2u hp = *(const v2u*)&hn[32 * w + 2 * r16];
        float ag[8], au[8];
#pragma unroll
        for (int e = 0; e < 8; ++e) {
          ag[e] = dot2(gp[1][e], hp.y, dot2(gp[0][e], hp.x, 0.f));
          au[e] = dot2(up[1][e], hp.y, dot2(up[0][e], hp.x, 0.f));
        }
        float s[8];
#pragma unroll
        for (int k = 0; k < 8; ++k) s[k] = sw32(ag[k], au[k]);
        v4f t;
#pragma unroll
        for (int k = 0; k < 4; ++k) {
          float a = sw16(s[k], s[k + 4]);
          a += dppf<R8>(a);
          a += dppf<R4>(a);
          t[k] = a;
        }
        if ((lane & 12) == 0) *(v4f*)&red[w * 64 + (g >> 1) * 32 + 8 * c4 + 4 * (g & 1)] = t;
      }
      bar();
      TS(9);
      {
        v4f gg = *(const v4f*)&red[c8 * 64 + 4 * r8], uu = *(const v4f*)&red[c8 * 64 + 32 + 4 * r8];
        float hh[4];
#pragma unroll
        for (int k = 0; k < 4; ++k) {
          float a = gg[k], d = uu[k];
          a += dppf<X1>(a); d += dppf<X1>(d);
          a += dppf<X2>(a); d += dppf<X2>(d);
          a += dppf<HM>(a); d += dppf<HM>(d);
          hh[k] = rbf(a) * rbf(d);
        }
        const u32 h0 = pk(hh[0], hh[1]), h1 = pk(hh[2], hh[3]);
        float a[8];
#pragma unroll
        for (int e = 0; e < 8; ++e) a[e] = dot2(dp[1][e], h1, dot2(dp[0][e], h0, 0.f));
        u32 p01, p23;
        colred(a, p01, p23);
        const int cg = 16 * w + 2 * c8 + (lane >> 5);
        if ((lane & 24) == 0) pub(rws, OFF_P + ((fb * 128 + cg) * 64 + j) * 16, p01, t0, p23, t1);
      }
      TS(10);
    }
  }
}

extern "C" void kernel_launch(void* const* d_in, const int* in_sizes, int n_in, void* d_out, int out_size, void* d_ws,
                              size_t ws_size, hipStream_t stream) {
  fused_decode<<<256, 512, 0, stream>>>((const u16*)d_in[0], (const u16*)d_in[1], (const u16*)d_in[2], (const u16*)d_in[3],
                                        (const u16*)d_in[4], (const u16*)d_in[6], (const u16*)d_in[7], (const u16*)d_in[8],
                                        (u16*)d_out, (unsigned char*)d_ws);
}
```

```cpp
#include <hip/hip_runtime.h>
#include <stdint.h>
#include <stdio.h>
#define TSTAMP 1

typedef unsigned short u16;
typedef unsigned u32;
typedef u32 v4u __attribute__((ext_vector_type(4)));
typedef u32 v2u __attribute__((ext_vector_type(2)));
typedef float v4f __attribute__((ext_vector_type(4)));
typedef float v2f __attribute__((ext_vector_type(2)));
typedef __bf16 bf2 __attribute__((ext_vector_type(2)));
typedef __bf16 bf8 __attribute__((ext_vector_type(8)));
typedef __amdgpu_buffer_rsrc_t rsrc_t;
#define DI __device__ __forceinline__
#define SB() __builtin_amdgcn_sched_barrier(0)

constexpr int D = 512, NH = 32, SK = 4096, DH = 128, WIN = 1024, F = 1376;
constexpr int NJ = F / 32;
constexpr u32 OFF_ML = 4096;
constexpr u32 OFF_H = 8192;
constexpr u32 OFF_Q = 16384;
constexpr u32 OFF_Y = OFF_Q + (1u << 20);
constexpr u32 OFF_P = OFF_Y + (1u << 19);
constexpr u32 WS_BYTES = OFF_P + (1u << 19);
constexpr int NT = 2, SC1 = 16;
constexpr int SPIN = 400000;
constexpr u32 PLO = 0x05040100u, PHI = 0x07060302u;
constexpr float LOG2E = 1.4426950408889634f;

#define TS(i) do { SB(); ts[i] = __builtin_amdgcn_s_memrealtime(); asm volatile("" : "+s"(ts[i])); SB(); } while (0)

DI float f_(u32 u) { return __builtin_bit_cast(float, u); }
DI u32 u_(float f) { return __builtin_bit_cast(u32, f); }
DI float blo(u32 u) { return f_(u << 16); }
DI float bhi(u32 u) { return f_(u & 0xffff0000u); }
DI u32 pk(float a, float b) {
  v2f v = {a, b};
  return __builtin_bit_cast(u32, __builtin_convertvector(v, bf2));
}
DI float rbf(float a) { return f_(pk(a, a) & 0xffff0000u); }
DI float dot2(u32 a, u32 b, float c) {
  return __builtin_amdgcn_fdot2_f32_bf16(__builtin_bit_cast(bf2, a), __builtin_bit_cast(bf2, b), c, false);
}
DI u32 prm(u32 second, u32 first, u32 sel) { return __builtin_amdgcn_perm(second, first, sel); }
template <int C> DI u32 dppu(u32 v) { return (u32)__builtin_amdgcn_mov_dpp((int)v, C, 0xf, 0xf, true); }
template <int C> DI float dppf(float v) { return f_(dppu<C>(u_(v))); }
constexpr int X1 = 0xB1, X2 = 0x4E, HM = 0x141, RM = 0x140, R4 = 0x124, R8 = 0x128;
DI float sw32(float a, float b) {
  auto r = __builtin_amdgcn_permlane32_swap(u_(a), u_(b), false, false);
  return f_(r[0]) + f_(r[1]);
}
DI float sw16(float a, float b) {
  auto r = __builtin_amdgcn_permlane16_swap(u_(a), u_(b), false, false);
  return f_(r[0]) + f_(r[1]);
}
DI float mx32(float a) {
  auto r = __builtin_amdgcn_permlane32_swap(u_(a), u_(a), false, false);
  return fmaxf(f_(r[0]), f_(r[1]));
}
DI float mx16(float a) {
  auto r = __builtin_amdgcn_permlane16_swap(u_(a), u_(a), false, false);
  return fmaxf(f_(r[0]), f_(r[1]));
}
DI float rowsum(float v) {
  v += dppf<X1>(v); v += dppf<X2>(v); v += dppf<HM>(v); v += dppf<RM>(v);
  return v;
}
DI float wavesum(float v) { v = rowsum(v); v = sw16(v, v); return sw32(v, v); }
DI float ex2(float v) { return __builtin_amdgcn_exp2f(v); }
DI float rcp(float v) { return __builtin_amdgcn_rcpf(v); }

DI rsrc_t mkr(const void* p, u32 bytes) { return __builtin_amdgcn_make_buffer_rsrc((void*)p, 0, bytes, 0x00020000); }
template <int AUX> DI v4u ld(rsrc_t r, u32 voff, u32 soff) { return __builtin_amdgcn_raw_buffer_load_b128(r, voff, soff, AUX); }
DI void pub(rsrc_t r, u32 off, u32 a, u32 t0, u32 b, u32 t1) {
  v4u v = {a, t0, b, t1};
  __builtin_amdgcn_raw_buffer_store_b128(v, r, off, 0, SC1);
}
DI void bar() { asm volatile("s_waitcnt lgkmcnt(0)\n\ts_barrier" ::: "memory"); }

template <int N> DI void poll(v4u (&v)[N], rsrc_t r, u32 voff, u32 stride, u32 t0, u32 t1) {
  for (int it = 0; it < SPIN; ++it) {
    asm volatile("" ::: "memory");
#pragma unroll
    for (int n = 0; n < N; ++n) v[n] = ld<SC1>(r, voff, n * stride);
    bool ok = true;
#pragma unroll
    for (int n = 0; n < N; ++n) ok = ok && (v[n].y == t0) && (v[n].w == t1);
    if (__all(ok)) break;
  }
}

DI float ln_rinv(float var) { return rcp(rbf(__builtin_amdgcn_sqrtf(rbf(rbf(var) + 1e-5f)))); }
DI u32 ln_pair(u32 v, float m, float rinv) { return pk(rbf(blo(v) - m) * rinv, rbf(bhi(v) - m) * rinv); }

DI void colred(const float (&a)[8], u32& p01, u32& p23) {
  float s[4];
#pragma unroll
  for (int k = 0; k < 4; ++k) s[k] = sw32(a[k], a[k + 4]);
#pragma unroll
  for (int k = 0; k < 4; ++k) s[k] = sw16(s[k], s[k]);
#pragma unroll
  for (int k = 0; k < 4; ++k) s[k] += dppf<R8>(s[k]);
  p01 = pk(s[0], s[1]);
  p23 = pk(s[2], s[3]);
}
DI float red4(float v0, float v1, float v2, float v3) {
  float s0 = sw32(v0, v2), s1 = sw32(v1, v3);
  float t = sw16(s0, s1);
  return rowsum(t);
}

__global__ void __launch_bounds__(512, 2)
fused_decode(const u16* __restrict__ x, const u16* __restrict__ kc, const u16* __restrict__ vc, const u16* __restrict__ wq,
             const u16* __restrict__ wo, const u16* __restrict__ wg, const u16* __restrict__ wu,
             const u16* __restrict__ wd, u16* __restrict__ out, unsigned char* __restrict__ ws) {
  __shared__ __attribute__((aligned(16))) u32 qs[64];
  __shared__ __attribute__((aligned(16))) float ml[16];
  __shared__ __attribute__((aligned(16))) float op[8 * 128];
  __shared__ __attribute__((aligned(16))) u32 ob[64];
  __shared__ __attribute__((aligned(16))) u32 hn[256];
  __shared__ __attribute__((aligned(16))) float red[8 * 64];
  unsigned long long ts[16] = {};
  TS(0);
  const int blk = blockIdx.x, xc = blk & 7, ii = blk >> 3;
  const int h = 4 * xc + (ii & 3), r = ii >> 2, b = r >> 1, half = r & 1;
  const int tid = threadIdx.x, lane = tid & 63, w = __builtin_amdgcn_readfirstlane(tid >> 6);
  const int g = lane >> 4, c = lane & 15, r8 = lane >> 3, c8 = lane & 7;

  asm volatile("" ::"s"(x), "s"(kc), "s"(vc), "s"(wq), "s"(wo), "s"(wg), "s"(wu), "s"(wd), "s"(out), "s"(ws));
  const rsrc_t rws = mkr(ws, WS_BYTES);
  v2u ep = __builtin_amdgcn_raw_buffer_load_b64(rws, 0, 0, SC1);
  u32 ta = 0, tb = 0;
  const size_t kvbase = ((size_t)(b * NH + h) * SK + (SK - WIN) + half * 512) * DH;
  const rsrc_t rk = mkr(kc + kvbase, 512 * DH * 2);
  const rsrc_t rv = mkr(vc + kvbase, 512 * DH * 2);
  const rsrc_t rwo = mkr(wo + (size_t)h * DH * D, DH * D * 2);
  const rsrc_t rx = mkr(x, 4 * D * 2);
  const u32 kvoff = (64 * w + g) * 256 + 16 * c;
  const u32 wooff = 16 * r8 * 1024 + 128 * w + 16 * c8;

  if (w == 0) {
    const rsrc_t rq = mkr(wq + (size_t)64 * r * (NH * DH) + h * DH, 64 * NH * DH * 2 - h * DH * 2);
    const int bb = c >> 2, sub = (c & 3) + 4 * g;
    v4u xs[4], xa[2], q[16];
#pragma unroll
    for (int k = 0; k < 4; ++k) xs[k] = ld<0>(rx, bb * 1024 + 16 * sub, 256 * k);
#pragma unroll
    for (int k = 0; k < 2; ++k) xa[k] = ld<0>(rx, bb * 1024 + 128 * r + 32 * g, 16 * k);
    SB();
#pragma unroll
    for (int i = 0; i < 16; ++i) q[i] = ld<NT>(rq, 16 * g * 8192 + 16 * c, i * 8192);
    SB();
    TS(15);
    asm volatile("" : "+v"(xs[0]), "+v"(xs[1]), "+v"(xs[2]), "+v"(xs[3]));
    TS(12);
    float s = 0.f;
#pragma unroll
    for (int k = 0; k < 4; ++k)
#pragma unroll
      for (int e = 0; e < 4; ++e) s += blo(xs[k][e]) + bhi(xs[k][e]);
    s += dppf<X1>(s); s += dppf<X2>(s);
    s = sw16(s, s); s = sw32(s, s);
    const float mean = s * (1.f / D);
    float vs = 0.f;
#pragma unroll
    for (int k = 0; k < 4; ++k)
#pragma unroll
      for (int e = 0; e < 4; ++e) {
        float d0 = blo(xs[k][e]) - mean, d1 = bhi(xs[k][e]) - mean;
        vs = fmaf(d0, d0, vs); vs = fmaf(d1, d1, vs);
      }
    vs += dppf<X1>(vs); vs += dppf<X2>(vs);
    vs = sw16(vs, vs); vs = sw32(vs, vs);
    const float mb = rbf(mean), rinv = ln_rinv(vs * (1.f / D));
    TS(14);
    v4u an[2];
#pragma unroll
    for (int k = 0; k < 2; ++k)
#pragma unroll
      for (int e = 0; e < 4; ++e) an[k][e] = ln_pair(xa[k][e], mb, rinv);
    float qa[8];
#pragma unroll
    for (int j = 0; j < 8; ++j) {
      v4f acc = {0.f, 0.f, 0.f, 0.f};
#pragma unroll
      for (int k = 0; k < 2; ++k) {
        v4u bq;
#pragma unroll
        for (int t = 0; t < 4; ++t) bq[t] = prm(q[8 * k + 2 * t + 1][j >> 1], q[8 * k + 2 * t][j >> 1], (j & 1) ? PHI : PLO);
        acc = __builtin_amdgcn_mfma_f32_16x16x32_bf16(__builtin_bit_cast(bf8, an[k]), __builtin_bit_cast(bf8, bq), acc, 0, 0, 0);
      }
      qa[j] = acc[0];
    }
    asm volatile("" : "+v"(qa[0]), "+v"(qa[1]), "+v"(qa[2]), "+v"(qa[3]), "+v"(qa[4]), "+v"(qa[5]), "+v"(qa[6]), "+v"(qa[7]));
    TS(13);
    ta = ep.x; tb = ep.y;
    asm volatile("" : "+v"(ta), "+v"(tb));
    ta += 1u; tb += 1u;
    const u32 qo = OFF_Q + (((h * 4 + g) * 8 + r) * 64 + c) * 16;
#pragma unroll
    for (int t = 0; t < 4; ++t) pub(rws, qo + 256 * t, u_(qa[2 * t]), ta, u_(qa[2 * t + 1]), tb);
    SB();
  }
  TS(1);

  v4u kk[16], vv[16], ww[16];
#pragma unroll
  for (int i = 0; i < 16; ++i) kk[i] = ld<NT>(rk, kvoff, i * 1024);
  SB();
  if (w == 0) {
    v4u qp[8];
    poll<8>(qp, rws, OFF_Q + ((h * 4 + b) * 8 * 64 + lane) * 16, 1024, ta, tb);
    float q0 = 0.f, q1 = 0.f;
#pragma unroll
    for (int n = 0; n < 8; ++n) { q0 += f_(qp[n].x); q1 += f_(qp[n].z); }
    qs[4 * c + g] = pk(q0, q1);
    SB();
  }
#pragma unroll
  for (int i = 0; i < 4; ++i) { vv[i] = ld<NT>(rv, kvoff, i * 1024); ww[i] = ld<NT>(rwo, wooff, i * 1024); }
  SB();
  u32 t0 = ep.x, t1 = ep.y;
  asm volatile("" : "+v"(t0), "+v"(t1));
  t0 += 1u; t1 += 1u;
  bar();
  TS(2);

  float p, mw;
  {
    const v4u qv = *(const v4u*)&qs[4 * c];
    float s[16];
#pragma unroll
    for (int i = 0; i < 16; ++i) {
      float a = dot2(kk[i].x, qv.x, 0.f);
      a = dot2(kk[i].y, qv.y, a);
      a = dot2(kk[i].z, qv.z, a);
      s[i] = dot2(kk[i].w, qv.w, a);
    }
    const bool b3 = c & 8, b2 = c & 4, b1 = c & 2, b0 = c & 1;
#pragma unroll
    for (int i = 0; i < 8; ++i) s[i] = (b3 ? s[i + 8] : s[i]) + dppf<RM>(b3 ? s[i] : s[i + 8]);
#pragma unroll
    for (int i = 0; i < 4; ++i) s[i] = (b2 ? s[i + 4] : s[i]) + dppf<HM>(b2 ? s[i] : s[i + 4]);
#pragma unroll
    for (int i = 0; i < 2; ++i) s[i] = (b1 ? s[i + 2] : s[i]) + dppf<X2>(b1 ? s[i] : s[i + 2]);
    const float raw = (b0 ? s[1] : s[0]) + dppf<X1>(b0 ? s[0] : s[1]);
    const float sc = rbf(rbf(raw) * 0.08838834764831845f);
    float m = sc;
    m = fmaxf(m, dppf<X1>(m)); m = fmaxf(m, dppf<X2>(m)); m = fmaxf(m, dppf<HM>(m)); m = fmaxf(m, dppf<RM>(m));
    m = mx32(mx16(m));
    mw = m;
    p = ex2((sc - m) * LOG2E);
    const float l = wavesum(p);
    if (lane == 0) { v2f t = {m, l}; *(v2f*)&ml[2 * w] = t; }
  }
  bar();
  TS(3);
  float fw;
  {
    const v2f t = *(const v2f*)&ml[2 * c8];
    float M = t.x;
    M = fmaxf(M, dppf<X1>(M)); M = fmaxf(M, dppf<X2>(M)); M = fmaxf(M, dppf<HM>(M));
    float L = t.y * ex2((t.x - M) * LOG2E);
    L += dppf<X1>(L); L += dppf<X2>(L); L += dppf<HM>(L);
    fw = ex2((mw - M) * LOG2E) * rcp(L);
    if (w == 0 && lane == 0) pub(rws, OFF_ML + (b * 64 + h * 2 + half) * 16, u_(M), t0, u_(L), t1);
  }
  const u32 ppk = pk(p, dppf<X1>(p));
  SB();

  float acc[8] = {0.f, 0.f, 0.f, 0.f, 0.f, 0.f, 0.f, 0.f};
  u32 wp[8][8];
#define PVSTEP(t)                                                                       \
  do {                                                                                  \
    const u32 pp = dppu<0x150 + 2 * (t)>(ppk);                                          \
    _Pragma("unroll") for (int e = 0; e < 4; ++e) {                                     \
      acc[2 * e] = dot2(prm(vv[2 * (t) + 1][e], vv[2 * (t)][e], PLO), pp, acc[2 * e]);  \
      acc[2 * e + 1] = dot2(prm(vv[2 * (t) + 1][e], vv[2 * (t)][e], PHI), pp, acc[2 * e + 1]); \
      wp[t][2 * e] = prm(ww[2 * (t) + 1][e], ww[2 * (t)][e], PLO);                      \
      wp[t][2 * e + 1] = prm(ww[2 * (t) + 1][e], ww[2 * (t)][e], PHI);                  \
    }                                                                                   \
    _Pragma("unroll") for (int e = 0; e < 8; ++e) asm volatile("" : "+v"(wp[t][e]));    \
    SB();                                                                               \
  } while (0)
#define VWLD(i) do { vv[i] = ld<NT>(rv, kvoff, (i) * 1024); ww[i] = ld<NT>(rwo, wooff, (i) * 1024); } while (0)
  VWLD(4); VWLD(5); VWLD(6); VWLD(7); SB();
  PVSTEP(0);
  VWLD(8); VWLD(9); SB();
  PVSTEP(1);
  VWLD(10); VWLD(11); SB();
  PVSTEP(2);
  VWLD(12); VWLD(13); SB();
  PVSTEP(3);
  VWLD(14); VWLD(15); SB();
  PVSTEP(4); PVSTEP(5); PVSTEP(6); PVSTEP(7);
  TS(4);
  {
    float s[4];
#pragma unroll
    for (int k = 0; k < 4; ++k) s[k] = sw32(acc[k], acc[k + 4]);
    v2f o = {sw16(s[0], s[2]) * fw, sw16(s[1], s[3]) * fw};
    *(v2f*)&op[w * 128 + 8 * c + 2 * g] = o;
  }
  bar();
  {
    const v2f t = *(const v2f*)&op[r8 * 128 + 16 * w + 2 * c8];
    float a = t.x + dppf<R8>(t.x), d = t.y + dppf<R8>(t.y);
    a = sw16(a, a); d = sw16(d, d);
    a = sw32(a, a); d = sw32(d, d);
    if (lane < 8) ob[8 * w + lane] = pk(a, d);
  }
  bar();
  TS(5);
  {
    const v4u o0 = *(const v4u*)&ob[8 * r8], o1 = *(const v4u*)&ob[8 * r8 + 4];
    float a[8] = {0.f, 0.f, 0.f, 0.f, 0.f, 0.f, 0.f, 0.f};
#pragma unroll
    for (int t = 0; t < 8; ++t)
#pragma unroll
      for (int e = 0; e < 8; ++e) a[e] = dot2(wp[t][e], t < 4 ? o0[t & 3] : o1[t & 3], a[e]);
    u32 p01, p23;
    colred(a, p01, p23);
    const int cg = 16 * w + 2 * c8 + (lane >> 5);
    if ((lane & 24) == 0) pub(rws, OFF_Y + ((b * 128 + cg) * 64 + h * 2 + half) * 16, p01, t0, p23, t1);
  }
  TS(6);

  if (ii >= 24) {
    const int ri = xc + 8 * (ii - 24), rb = ri >> 4, cg = 8 * (ri & 15) + w;
    const u32 xraw = x[rb * D + 4 * cg + g];
    v4u mlv[1], yv[1];
    poll<1>(mlv, rws, OFF_ML + (rb * 64 + lane) * 16, 0, t0, t1);
    const float M = f_(mlv[0].x), L = f_(mlv[0].z);
    const float Mo = dppf<X1>(M), Lo = dppf<X1>(L);
    const float Ms = fmaxf(M, Mo);
    const float e = L * ex2((M - Ms) * LOG2E), eo = Lo * ex2((Mo - Ms) * LOG2E);
    const float wt = e * rcp(e + eo);
    TS(7);
    poll<1>(yv, rws, OFF_Y + ((rb * 128 + cg) * 64 + lane) * 16, 0, t0, t1);
    TS(8);
    const float y = red4(blo(yv[0].x) * wt, bhi(yv[0].x) * wt, blo(yv[0].z) * wt, bhi(yv[0].z) * wt);
    const float hv = rbf(blo(xraw) + rbf(y));
    {
      auto a = __builtin_amdgcn_permlane16_swap(u_(hv), u_(hv), false, false);
      const u32 hp = prm(a[1], a[0], PHI);
      auto d = __builtin_amdgcn_permlane32_swap(hp, hp, false, false);
      if (lane == 0) pub(rws, OFF_H + (rb * 128 + cg) * 16, d[0], t0, d[1], t1);
    }
    TS(9);
    if (ri == 0 && w == 0) {
      v4u hv3[1];
      poll<1>(hv3, rws, OFF_H + ((lane & 3) * 128) * 16, 0, t0, t1);
    }
    v4u pv[1];
    const int j = lane < NJ ? lane : NJ - 1;
    poll<1>(pv, rws, OFF_P + ((rb * 128 + cg) * 64 + j) * 16, 0, t0, t1);
    TS(10);
    const bool on = lane < NJ;
    const float dn = red4(on ? blo(pv[0].x) : 0.f, on ? bhi(pv[0].x) : 0.f, on ? blo(pv[0].z) : 0.f, on ? bhi(pv[0].z) : 0.f);
    const u32 o = pk(hv + rbf(dn), 0.f);
    if (c == 0) out[rb * D + 4 * cg + g] = (u16)o;
    if (ri == 0 && w == 0 && lane == 0) {
      v2u e2 = {t0, t1};
      __builtin_amdgcn_raw_buffer_store_b64(e2, rws, 0, 0, SC1);
    }
    TS(11);
  } else {
    const int j = 2 * (xc + 8 * (ii >> 3)) + ((ii >> 2) & 1), fb = ii & 3;
    if (j < NJ) {
      const int r16 = lane >> 2, c4 = lane & 3;
      const rsrc_t rg = mkr(wg + 32 * j, D * F * 2 - 64 * j);
      const rsrc_t ru = mkr(wu + 32 * j, D * F * 2 - 64 * j);
      const rsrc_t rd = mkr(wd + (size_t)32 * j * D, 32 * D * 2);
      const u32 goff = (64 * w + 4 * r16) * (F * 2) + 16 * c4;
      const u32 doff = 4 * r8 * 1024 + 128 * w + 16 * c8;
      v4u gw[4], uw[4], dw[4];
#pragma unroll
      for (int i = 0; i < 4; ++i) { gw[i] = ld<NT>(rg, goff, i * F * 2); uw[i] = ld<NT>(ru, goff, i * F * 2); }
#pragma unroll
      for (int i = 0; i < 4; ++i) dw[i] = ld<NT>(rd, doff, i * 1024);
      SB();
      if (w == 0) {
        v4u hv[2];
        poll<2>(hv, rws, OFF_H + (fb * 128 + lane) * 16, 1024, t0, t1);
        TS(7);
        float v[8] = {blo(hv[0].x), bhi(hv[0].x), blo(hv[0].z), bhi(hv[0].z), blo(hv[1].x), bhi(hv[1].x), blo(hv[1].z), bhi(hv[1].z)};
        const float mean = wavesum(((v[0] + v[1]) + (v[2] + v[3])) + ((v[4] + v[5]) + (v[6] + v[7]))) * (1.f / D);
        float vs = 0.f;
#pragma unroll
        for (int k = 0; k < 8; ++k) { float d = v[k] - mean; vs = fmaf(d, d, vs); }
        const float mb = rbf(mean), rinv = ln_rinv(wavesum(vs) * (1.f / D));
        v2u a = {ln_pair(hv[0].x, mb, rinv), ln_pair(hv[0].z, mb, rinv)};
        v2u d = {ln_pair(hv[1].x, mb, rinv), ln_pair(hv[1].z, mb, rinv)};
        *(v2u*)&hn[2 * lane] = a;
        *(v2u*)&hn[128 + 2 * lane] = d;
      }
      u32 gp[2][8], up[2][8], dp[2][8];
#pragma unroll
      for (int t = 0; t < 2; ++t)
#pragma unroll
        for (int e = 0; e < 4; ++e) {
          gp[t][2 * e] = prm(gw[2 * t + 1][e], gw[2 * t][e], PLO); gp[t][2 * e + 1] = prm(gw[2 * t + 1][e], gw[2 * t][e], PHI);
          up[t][2 * e] = prm(uw[2 * t + 1][e], uw[2 * t][e], PLO); up[t][2 * e + 1] = prm(uw[2 * t + 1][e], uw[2 * t][e], PHI);
          dp[t][2 * e] = prm(dw[2 * t + 1][e], dw[2 * t][e], PLO); dp[t][2 * e + 1] = prm(dw[2 * t + 1][e], dw[2 * t][e], PHI);
        }
#pragma unroll
      for (int t = 0; t < 2; ++t)
#pragma unroll
        for (int e = 0; e < 8; ++e) asm volatile("" : "+v"(gp[t][e]), "+v"(up[t][e]), "+v"(dp[t][e]));
      bar();
      TS(8);
      {
        const v2u hp = *(const v2u*)&hn[32 * w + 2 * r16];
        float ag[8], au[8];
#pragma unroll
        for (int e = 0; e < 8; ++e) {
          ag[e] = dot2(gp[1][e], hp.y, dot2(gp[0][e], hp.x, 0.f));
          au[e] = dot2(up[1][e], hp.y, dot2(up[0][e], hp.x, 0.f));
        }
        float s[8];
#pragma unroll
        for (int k = 0; k < 8; ++k) s[k] = sw32(ag[k], au[k]);
        v4f t;
#pragma unroll
        for (int k = 0; k < 4; ++k) {
          float a = sw16(s[k], s[k + 4]);
          a += dppf<R8>(a);
          a += dppf<R4>(a);
          t[k] = a;
        }
        if ((lane & 12) == 0) *(v4f*)&red[w * 64 + (g >> 1) * 32 + 8 * c4 + 4 * (g & 1)] = t;
      }
      bar();
      TS(9);
      {
        v4f gg = *(const v4f*)&red[c8 * 64 + 4 * r8], uu = *(const v4f*)&red[c8 * 64 + 32 + 4 * r8];
        float hh[4];
#pragma unroll
        for (int k = 0; k < 4; ++k) {
          float a = gg[k], d = uu[k];
          a += dppf<X1>(a); d += dppf<X1>(d);
          a += dppf<X2>(a); d += dppf<X2>(d);
          a += dppf<HM>(a); d += dppf<HM>(d);
          hh[k] = rbf(a) * rbf(d);
        }
        const u32 h0 = pk(hh[0], hh[1]), h1 = pk(hh[2], hh[3]);
        float a[8];
#pragma unroll
        for (int e = 0; e < 8; ++e) a[e] = dot2(dp[1][e], h1, dot2(dp[0][e], h0, 0.f));
        u32 p01, p23;
        colred(a, p01, p23);
        const int cg = 16 * w + 2 * c8 + (lane >> 5);
        if ((lane & 24) == 0) pub(rws, OFF_P + ((fb * 128 + cg) * 64 + j) * 16, p01, t0, p23, t1);
      }
      TS(10);
    }
  }
  if (lane == 0 && w == 0 && (blk == 250 || blk == 3)) {
    printf("TL blk %d t0 %llu: q13 %d q1 %d bq %d sc %d pv %d c %d y %d t7 %d t8 %d t9 %d t10 %d t11 %d\n", blk, ts[0] % 100000000ull, (int)(ts[13]-ts[0]), (int)(ts[1]-ts[0]), (int)(ts[2]-ts[0]), (int)(ts[3]-ts[0]),
      (int)(ts[4]-ts[0]), (int)(ts[5]-ts[0]), (int)(ts[6]-ts[0]), (int)(ts[7]-ts[0]), (int)(ts[8]-ts[0]), (int)(ts[9]-ts[0]), (int)(ts[10]-ts[0]), (int)(ts[11]-ts[0]));
  }
}

extern "C" void kernel_launch(void* const* d_in, const int* in_sizes, int n_in, void* d_out, int out_size, void* d_ws,
                              size_t ws_size, hipStream_t stream) {
  fused_decode<<<256, 512, 0, stream>>>((const u16*)d_in[0], (const u16*)d_in[1], (const u16*)d_in[2], (const u16*)d_in[3],
                                        (const u16*)d_in[4], (const u16*)d_in[6], (const u16*)d_in[7], (const u16*)d_in[8],
                                        (u16*)d_out, (unsigned char*)d_ws);
}
```

```cpp
#define YBAR
#include <hip/hip_runtime.h>
#include <stdint.h>

typedef unsigned short u16;
typedef unsigned u32;
typedef u32 v4u __attribute__((ext_vector_type(4)));
typedef u32 v2u __attribute__((ext_vector_type(2)));
typedef float v4f __attribute__((ext_vector_type(4)));
typedef float v2f __attribute__((ext_vector_type(2)));
typedef __bf16 bf2 __attribute__((ext_vector_type(2)));
typedef __bf16 bf8 __attribute__((ext_vector_type(8)));
typedef __amdgpu_buffer_rsrc_t rsrc_t;
#define DI __device__ __forceinline__
#define SB() __builtin_amdgcn_sched_barrier(0)

constexpr int D = 512, NH = 32, SK = 4096, DH = 128, WIN = 1024, F = 1376;
constexpr int NJ = F / 32;
constexpr u32 OFF_ML = 4096;
constexpr u32 OFF_H = 8192;
constexpr u32 OFF_Q = 16384;
constexpr u32 OFF_Y = OFF_Q + (1u << 20);
constexpr u32 OFF_P = OFF_Y + (1u << 19);
constexpr u32 WS_BYTES = OFF_P + (1u << 19);
constexpr int NT = 2, SC1 = 16;
#define FAUX NT
constexpr int SPIN = 400000;
constexpr u32 PLO = 0x05040100u, PHI = 0x07060302u;
constexpr float LOG2E = 1.4426950408889634f;

#define TS(i)

DI float f_(u32 u) { return __builtin_bit_cast(float, u); }
DI u32 u_(float f) { return __builtin_bit_cast(u32, f); }
DI float blo(u32 u) { return f_(u << 16); }
DI float bhi(u32 u) { return f_(u & 0xffff0000u); }
DI u32 pk(float a, float b) {
  v2f v = {a, b};
  return __builtin_bit_cast(u32, __builtin_convertvector(v, bf2));
}
DI float rbf(float a) { return f_(pk(a, a) & 0xffff0000u); }
DI float dot2(u32 a, u32 b, float c) {
  return __builtin_amdgcn_fdot2_f32_bf16(__builtin_bit_cast(bf2, a), __builtin_bit_cast(bf2, b), c, false);
}
DI u32 prm(u32 second, u32 first, u32 sel) { return __builtin_amdgcn_perm(second, first, sel); }
template <int C> DI u32 dppu(u32 v) { return (u32)__builtin_amdgcn_mov_dpp((int)v, C, 0xf, 0xf, true); }
template <int C> DI float dppf(float v) { return f_(dppu<C>(u_(v))); }
constexpr int X1 = 0xB1, X2 = 0x4E, HM = 0x141, RM = 0x140, R4 = 0x124, R8 = 0x128;
DI float sw32(float a, float b) {
  auto r = __builtin_amdgcn_permlane32_swap(u_(a), u_(b), false, false);
  return f_(r[0]) + f_(r[1]);
}
DI float sw16(float a, float b) {
  auto r = __builtin_amdgcn_permlane16_swap(u_(a), u_(b), false, false);
  return f_(r[0]) + f_(r[1]);
}
DI float mx32(float a) {
  auto r = __builtin_amdgcn_permlane32_swap(u_(a), u_(a), false, false);
  return fmaxf(f_(r[0]), f_(r[1]));
}
DI float mx16(float a) {
  auto r = __builtin_amdgcn_permlane16_swap(u_(a), u_(a), false, false);
  return fmaxf(f_(r[0]), f_(r[1]));
}
DI float rowsum(float v) {
  v += dppf<X1>(v); v += dppf<X2>(v); v += dppf<HM>(v); v += dppf<RM>(v);
  return v;
}
DI float wavesum(float v) { v = rowsum(v); v = sw16(v, v); return sw32(v, v); }
DI float ex2(float v) { return __builtin_amdgcn_exp2f(v); }
DI float rcp(float v) { return __builtin_amdgcn_rcpf(v); }

DI rsrc_t mkr(const void* p, u32 bytes) { return __builtin_amdgcn_make_buffer_rsrc((void*)p, 0, bytes, 0x00020000); }
template <int AUX> DI v4u ld(rsrc_t r, u32 voff, u32 soff) { return __builtin_amdgcn_raw_buffer_load_b128(r, voff, soff, AUX); }
DI void pub(rsrc_t r, u32 off, u32 a, u32 t0, u32 b, u32 t1) {
  v4u v = {a, t0, b, t1};
  __builtin_amdgcn_raw_buffer_store_b128(v, r, off, 0, SC1);
}
DI void bar() { asm volatile("s_waitcnt lgkmcnt(0)\n\ts_barrier" ::: "memory"); }

template <int N> DI void poll(v4u (&v)[N], rsrc_t r, u32 voff, u32 stride, u32 t0, u32 t1) {
  for (int it = 0; it < SPIN; ++it) {
    asm volatile("" ::: "memory");
#pragma unroll
    for (int n = 0; n < N; ++n) v[n] = ld<SC1>(r, voff, n * stride);
    bool ok = true;
#pragma unroll
    for (int n = 0; n < N; ++n) ok = ok && (v[n].y == t0) && (v[n].w == t1);
    if (__all(ok)) break;
  }
}

DI float ln_rinv(float var) { return rcp(rbf(__builtin_amdgcn_sqrtf(rbf(rbf(var) + 1e-5f)))); }
DI u32 ln_pair(u32 v, float m, float rinv) { return pk(rbf(blo(v) - m) * rinv, rbf(bhi(v) - m) * rinv); }

DI void colred(const float (&a)[8], u32& p01, u32& p23) {
  float s[4];
#pragma unroll
  for (int k = 0; k < 4; ++k) s[k] = sw32(a[k], a[k + 4]);
#pragma unroll
  for (int k = 0; k < 4; ++k) s[k] = sw16(s[k], s[k]);
#pragma unroll
  for (int k = 0; k < 4; ++k) s[k] += dppf<R8>(s[k]);
  p01 = pk(s[0], s[1]);
  p23 = pk(s[2], s[3]);
}
DI float red4(float v0, float v1, float v2, float v3) {
  float s0 = sw32(v0, v2), s1 = sw32(v1, v3);
  float t = sw16(s0, s1);
  return rowsum(t);
}

DI float red4b(float v0, float v1, float v2, float v3) {
  float s0 = sw32(v0, v2), s1 = sw32(v1, v3);
  float t = sw16(s0, s1);
  return t + dppf<R8>(t);
}

__global__ void __launch_bounds__(512, 2)
fused_decode(const u16* __restrict__ x, const u16* __restrict__ kc, const u16* __restrict__ vc, const u16* __restrict__ wq,
             const u16* __restrict__ wo, const u16* __restrict__ wg, const u16* __restrict__ wu,
             const u16* __restrict__ wd, u16* __restrict__ out, unsigned char* __restrict__ ws) {
  __shared__ __attribute__((aligned(16))) u32 qs[64];
  __shared__ __attribute__((aligned(16))) float ml[16];
  __shared__ __attribute__((aligned(16))) float op[8 * 128];
  __shared__ __attribute__((aligned(16))) u32 ob[64];
  __shared__ __attribute__((aligned(16))) u32 hn[256];
  __shared__ __attribute__((aligned(16))) float red[8 * 64];
  TS(0);
  const int blk = blockIdx.x, xc = blk & 7, ii = blk >> 3;
  const int h = 4 * xc + (ii & 3), r = ii >> 2, b = r >> 1, half = r & 1;
  const int tid = threadIdx.x, lane = tid & 63, w = __builtin_amdgcn_readfirstlane(tid >> 6);
  const int g = lane >> 4, c = lane & 15, r8 = lane >> 3, c8 = lane & 7;

  asm volatile("" ::"s"(x), "s"(kc), "s"(vc), "s"(wq), "s"(wo), "s"(wg), "s"(wu), "s"(wd), "s"(out), "s"(ws));
  const rsrc_t rws = mkr(ws, WS_BYTES);
  v2u ep = __builtin_amdgcn_raw_buffer_load_b64(rws, 0, 0, SC1);
  u32 ta = 0, tb = 0;
  const size_t kvbase = ((size_t)(b * NH + h) * SK + (SK - WIN) + half * 512) * DH;
  const rsrc_t rk = mkr(kc + kvbase, 512 * DH * 2);
  const rsrc_t rv = mkr(vc + kvbase, 512 * DH * 2);
  const rsrc_t rwo = mkr(wo + (size_t)h * DH * D, DH * D * 2);
  const rsrc_t rx = mkr(x, 4 * D * 2);
  const u32 kvoff = (64 * w + g) * 256 + 16 * c;
  const u32 wooff = 16 * r8 * 1024 + 128 * w + 16 * c8;

  if (w == 0) {
    const rsrc_t rq = mkr(wq + (size_t)64 * r * (NH * DH) + h * DH, 64 * NH * DH * 2 - h * DH * 2);
    const int bb = c >> 2, sub = (c & 3) + 4 * g;
    v4u xs[4], xa[2], q[16];
#pragma unroll
    for (int k = 0; k < 4; ++k) xs[k] = ld<0>(rx, bb * 1024 + 16 * sub, 256 * k);
#pragma unroll
    for (int k = 0; k < 2; ++k) xa[k] = ld<0>(rx, bb * 1024 + 128 * r + 32 * g, 16 * k);
    SB();
#pragma unroll
    for (int i = 0; i < 16; ++i) q[i] = ld<NT>(rq, 16 * g * 8192 + 16 * c, i * 8192);
    SB();
    TS(15);
    TS(12);
    float s = 0.f;
#pragma unroll
    for (int k = 0; k < 4; ++k)
#pragma unroll
      for (int e = 0; e < 4; ++e) s += blo(xs[k][e]) + bhi(xs[k][e]);
    s += dppf<X1>(s); s += dppf<X2>(s);
    s = sw16(s, s); s = sw32(s, s);
    const float mean = s * (1.f / D);
    float vs = 0.f;
#pragma unroll
    for (int k = 0; k < 4; ++k)
#pragma unroll
      for (int e = 0; e < 4; ++e) {
        float d0 = blo(xs[k][e]) - mean, d1 = bhi(xs[k][e]) - mean;
        vs = fmaf(d0, d0, vs); vs = fmaf(d1, d1, vs);
      }
    vs += dppf<X1>(vs); vs += dppf<X2>(vs);
    vs = sw16(vs, vs); vs = sw32(vs, vs);
    const float mb = rbf(mean), rinv = ln_rinv(vs * (1.f / D));
    TS(14);
    v4u an[2];
#pragma unroll
    for (int k = 0; k < 2; ++k)
#pragma unroll
      for (int e = 0; e < 4; ++e) an[k][e] = ln_pair(xa[k][e], mb, rinv);
    float qa[8];
#pragma unroll
    for (int j = 0; j < 8; ++j) {
      v4f acc = {0.f, 0.f, 0.f, 0.f};
#pragma unroll
      for (int k = 0; k < 2; ++k) {
        v4u bq;
#pragma unroll
        for (int t = 0; t < 4; ++t) bq[t] = prm(q[8 * k + 2 * t + 1][j >> 1], q[8 * k + 2 * t][j >> 1], (j & 1) ? PHI : PLO);
        acc = __builtin_amdgcn_mfma_f32_16x16x32_bf16(__builtin_bit_cast(bf8, an[k]), __builtin_bit_cast(bf8, bq), acc, 0, 0, 0);
      }
      qa[j] = acc[0];
    }
    TS(13);
    ta = ep.x; tb = ep.y;
    asm volatile("" : "+v"(ta), "+v"(tb));
    ta += 1u; tb += 1u;
    const u32 qo = OFF_Q + (((h * 4 + g) * 8 + r) * 64 + c) * 16;
#pragma unroll
    for (int t = 0; t < 4; ++t) pub(rws, qo + 256 * t, u_(qa[2 * t]), ta, u_(qa[2 * t + 1]), tb);
    SB();
  }
  TS(1);

  v4u kk[16], vv[16], ww[16];
#pragma unroll
  for (int i = 0; i < 16; ++i) kk[i] = ld<NT>(rk, kvoff, i * 1024);
  SB();
  if (w == 0) {
    v4u qp[8];
    poll<8>(qp, rws, OFF_Q + ((h * 4 + b) * 8 * 64 + lane) * 16, 1024, ta, tb);
    float q0 = 0.f, q1 = 0.f;
#pragma unroll
    for (int n = 0; n < 8; ++n) { q0 += f_(qp[n].x); q1 += f_(qp[n].z); }
    qs[4 * c + g] = pk(q0, q1);
    SB();
  }
#pragma unroll
  for (int i = 0; i < 4; ++i) { vv[i] = ld<NT>(rv, kvoff, i * 1024); ww[i] = ld<NT>(rwo, wooff, i * 1024); }
  SB();
  u32 t0 = ep.x, t1 = ep.y;
  asm volatile("" : "+v"(t0), "+v"(t1));
  t0 += 1u; t1 += 1u;
  bar();
  TS(2);

  float p, mw;
  {
    const v4u qv = *(const v4u*)&qs[4 * c];
    float s[16];
#pragma unroll
    for (int i = 0; i < 16; ++i) {
      float a = dot2(kk[i].x, qv.x, 0.f);
      a = dot2(kk[i].y, qv.y, a);
      a = dot2(kk[i].z, qv.z, a);
      s[i] = dot2(kk[i].w, qv.w, a);
    }
    const bool b3 = c & 8, b2 = c & 4, b1 = c & 2, b0 = c & 1;
#pragma unroll
    for (int i = 0; i < 8; ++i) s[i] = (b3 ? s[i + 8] : s[i]) + dppf<RM>(b3 ? s[i] : s[i + 8]);
#pragma unroll
    for (int i = 0; i < 4; ++i) s[i] = (b2 ? s[i + 4] : s[i]) + dppf<HM>(b2 ? s[i] : s[i + 4]);
#pragma unroll
    for (int i = 0; i < 2; ++i) s[i] = (b1 ? s[i + 2] : s[i]) + dppf<X2>(b1 ? s[i] : s[i + 2]);
    const float raw = (b0 ? s[1] : s[0]) + dppf<X1>(b0 ? s[0] : s[1]);
    const float sc = rbf(rbf(raw) * 0.08838834764831845f);
    float m = sc;
    m = fmaxf(m, dppf<X1>(m)); m = fmaxf(m, dppf<X2>(m)); m = fmaxf(m, dppf<HM>(m)); m = fmaxf(m, dppf<RM>(m));
    m = mx32(mx16(m));
    mw = m;
    p = ex2((sc - m) * LOG2E);
    const float l = wavesum(p);
    if (lane == 0) { v2f t = {m, l}; *(v2f*)&ml[2 * w] = t; }
  }
  bar();
  TS(3);
  float fw;
  {
    const v2f t = *(const v2f*)&ml[2 * c8];
    float M = t.x;
    M = fmaxf(M, dppf<X1>(M)); M = fmaxf(M, dppf<X2>(M)); M = fmaxf(M, dppf<HM>(M));
    float L = t.y * ex2((t.x - M) * LOG2E);
    L += dppf<X1>(L); L += dppf<X2>(L); L += dppf<HM>(L);
    fw = ex2((mw - M) * LOG2E) * rcp(L);
    if (w == 0 && lane == 0) pub(rws, OFF_ML + (b * 64 + h * 2 + half) * 16, u_(M), t0, u_(L), t1);
  }
  const u32 ppk = pk(p, dppf<X1>(p));
  SB();

  float acc[8] = {0.f, 0.f, 0.f, 0.f, 0.f, 0.f, 0.f, 0.f};
  u32 wp[8][8];
#define PVSTEP(t)                                                                       \
  do {                                                                                  \
    const u32 pp = dppu<0x150 + 2 * (t)>(ppk);                                          \
    _Pragma("unroll") for (int e = 0; e < 4; ++e) {                                     \
      acc[2 * e] = dot2(prm(vv[2 * (t) + 1][e], vv[2 * (t)][e], PLO), pp, acc[2 * e]);  \
      acc[2 * e + 1] = dot2(prm(vv[2 * (t) + 1][e], vv[2 * (t)][e], PHI), pp, acc[2 * e + 1]); \
      wp[t][2 * e] = prm(ww[2 * (t) + 1][e], ww[2 * (t)][e], PLO);                      \
      wp[t][2 * e + 1] = prm(ww[2 * (t) + 1][e], ww[2 * (t)][e], PHI);                  \
    }                                                                                   \
    _Pragma("unroll") for (int e = 0; e < 8; ++e) asm volatile("" : "+v"(wp[t][e]));    \
    SB();                                                                               \
  } while (0)
#define VWLD(i) do { vv[i] = ld<NT>(rv, kvoff, (i) * 1024); ww[i] = ld<NT>(rwo, wooff, (i) * 1024); } while (0)
  VWLD(4); VWLD(5); VWLD(6); VWLD(7); SB();
  PVSTEP(0);
  VWLD(8); VWLD(9); SB();
  PVSTEP(1);
  VWLD(10); VWLD(11); SB();
  PVSTEP(2);
  VWLD(12); VWLD(13); SB();
  PVSTEP(3);
  VWLD(14); VWLD(15); SB();
  PVSTEP(4); PVSTEP(5); PVSTEP(6); PVSTEP(7);
  TS(4);
  {
    float s[4];
#pragma unroll
    for (int k = 0; k < 4; ++k) s[k] = sw32(acc[k], acc[k + 4]);
    v2f o = {sw16(s[0], s[2]) * fw, sw16(s[1], s[3]) * fw};
    *(v2f*)&op[w * 128 + 8 * c + 2 * g] = o;
  }
  bar();
  {
    const v2f t = *(const v2f*)&op[r8 * 128 + 16 * w + 2 * c8];
    float a = t.x + dppf<R8>(t.x), d = t.y + dppf<R8>(t.y);
    a = sw16(a, a); d = sw16(d, d);
    a = sw32(a, a); d = sw32(d, d);
    if (lane < 8) ob[8 * w + lane] = pk(a, d);
  }
  bar();
  TS(5);
  {
    const v4u o0 = *(const v4u*)&ob[8 * r8], o1 = *(const v4u*)&ob[8 * r8 + 4];
    float a[8] = {0.f, 0.f, 0.f, 0.f, 0.f, 0.f, 0.f, 0.f};
#pragma unroll
    for (int t = 0; t < 8; ++t)
#pragma unroll
      for (int e = 0; e < 8; ++e) a[e] = dot2(wp[t][e], t < 4 ? o0[t & 3] : o1[t & 3], a[e]);
    u32 p01, p23;
    colred(a, p01, p23);
    const int cg = 16 * w + 2 * c8 + (lane >> 5);
    if ((lane & 24) == 0) pub(rws, OFF_Y + ((b * 64 + h * 2 + half) * 128 + cg) * 16, p01, t0, p23, t1);
  }
  TS(6);

  if (ii >= 24) {
    const int ri = xc + 8 * (ii - 24), rb = ri >> 4, cb = ri & 15;
    const u32 xraw = x[rb * D + 32 * cb + (lane & 31)];
    v4u mlv, yv;
    const u32 mlo = OFF_ML + (rb * 64 + 8 * w + r8) * 16;
    const u32 yo = OFF_Y + ((rb * 64 + 8 * w + r8) * 128 + 8 * cb + c8) * 16;
    {
      v4u a1[1];
      poll<1>(a1, rws, mlo, 0, t0, t1);
      mlv = a1[0];
    }
    const float M = f_(mlv.x), L = f_(mlv.z);
    const float Mo = dppf<R8>(M), Lo = dppf<R8>(L);
    const float Ms = fmaxf(M, Mo);
    const float e = L * ex2((M - Ms) * LOG2E), eo = Lo * ex2((Mo - Ms) * LOG2E);
    const float wt = e * rcp(e + eo);
    TS(7);
    {
      v4u a1[1];
      poll<1>(a1, rws, yo, 0, t0, t1);
      yv = a1[0];
    }
    TS(8);
    const int rslot = (4 * c8 + g) * 8 + w;
    {
      const float y = red4b(blo(yv.x) * wt, bhi(yv.x) * wt, blo(yv.z) * wt, bhi(yv.z) * wt);
      if (!(lane & 8)) red[rslot] = y;
    }
    bar();
    float hv = 0.f;
    if (w == 0) {
      const v4f t = *(const v4f*)&red[(lane & 31) * 8 + 4 * (lane >> 5)];
      const float s = (t[0] + t[1]) + (t[2] + t[3]);
      hv = rbf(blo(xraw) + rbf(sw32(s, s)));
      const u32 hp = prm(u_(dppf<X1>(hv)), u_(hv), PHI);
      const u32 ho = dppu<X2>(hp);
      if ((lane & 35) == 0) pub(rws, OFF_H + (rb * 128 + 8 * cb + (lane >> 2)) * 16, hp, t0, ho, t1);
    }
    TS(9);
    if (ri == 0 && w == 7) {
      v4u hv3[1];
      poll<1>(hv3, rws, OFF_H + ((lane & 3) * 128) * 16, 0, t0, t1);
      if (lane == 0) {
        v2u e2 = {t0, t1};
        __builtin_amdgcn_raw_buffer_store_b64(e2, rws, 0, 0, SC1);
      }
    }
    float dn = 0.f;
    if (w < 6) {
      v4u pv[1];
      const int j = 8 * w + r8;
      const bool on = j < NJ;
      poll<1>(pv, rws, OFF_P + ((rb * 64 + (on ? j : NJ - 1)) * 128 + 8 * cb + c8) * 16, 0, t0, t1);
      TS(10);
      dn = red4b(on ? blo(pv[0].x) : 0.f, on ? bhi(pv[0].x) : 0.f, on ? blo(pv[0].z) : 0.f, on ? bhi(pv[0].z) : 0.f);
    }
    if (!(lane & 8)) op[rslot] = dn;
    bar();
    if (w == 0) {
      const v4f t = *(const v4f*)&op[(lane & 31) * 8 + 4 * (lane >> 5)];
      const float s = (t[0] + t[1]) + (t[2] + t[3]);
      const float o = hv + rbf(sw32(s, s));
      const u32 po = pk(o, dppf<X1>(o));
      if ((lane & 33) == 0) *(u32*)&out[rb * D + 32 * cb + lane] = po;
    }
    TS(11);
  } else {
    const int j = 2 * (xc + 8 * (ii >> 3)) + ((ii >> 2) & 1), fb = ii & 3;
    if (j < NJ) {
      asm volatile("s_barrier" ::: "memory");
      const int r16 = lane >> 2, c4 = lane & 3;
      const rsrc_t rg = mkr(wg + 32 * j, D * F * 2 - 64 * j);
      const rsrc_t ru = mkr(wu + 32 * j, D * F * 2 - 64 * j);
      const rsrc_t rd = mkr(wd + (size_t)32 * j * D, 32 * D * 2);
      const u32 goff = (64 * w + 4 * r16) * (F * 2) + 16 * c4;
      const u32 doff = 4 * r8 * 1024 + 128 * w + 16 * c8;
      v4u gw[4], uw[4], dw[4];
#pragma unroll
      for (int i = 0; i < 4; ++i) { gw[i] = ld<FAUX>(rg, goff, i * F * 2); uw[i] = ld<FAUX>(ru, goff, i * F * 2); }
#pragma unroll
      for (int i = 0; i < 4; ++i) dw[i] = ld<FAUX>(rd, doff, i * 1024);
      SB();
      if (w == 0) {
        v4u hv[2];
        poll<2>(hv, rws, OFF_H + (fb * 128 + lane) * 16, 1024, t0, t1);
        TS(7);
        float v[8] = {blo(hv[0].x), bhi(hv[0].x), blo(hv[0].z), bhi(hv[0].z), blo(hv[1].x), bhi(hv[1].x), blo(hv[1].z), bhi(hv[1].z)};
        const float mean = wavesum(((v[0] + v[1]) + (v[2] + v[3])) + ((v[4] + v[5]) + (v[6] + v[7]))) * (1.f / D);
        float vs = 0.f;
#pragma unroll
        for (int k = 0; k < 8; ++k) { float d = v[k] - mean; vs = fmaf(d, d, vs); }
        const float mb = rbf(mean), rinv = ln_rinv(wavesum(vs) * (1.f / D));
        v2u a = {ln_pair(hv[0].x, mb, rinv), ln_pair(hv[0].z, mb, rinv)};
        v2u d = {ln_pair(hv[1].x, mb, rinv), ln_pair(hv[1].z, mb, rinv)};
        *(v2u*)&hn[2 * lane] = a;
        *(v2u*)&hn[128 + 2 * lane] = d;
      }
      u32 gp[2][8], up[2][8], dp[2][8];
#pragma unroll
      for (int t = 0; t < 2; ++t)
#pragma unroll
        for (int e = 0; e < 4; ++e) {
          gp[t][2 * e] = prm(gw[2 * t + 1][e], gw[2 * t][e], PLO); gp[t][2 * e + 1] = prm(gw[2 * t + 1][e], gw[2 * t][e], PHI);
          up[t][2 * e] = prm(uw[2 * t + 1][e], uw[2 * t][e], PLO); up[t][2 * e + 1] = prm(uw[2 * t + 1][e], uw[2 * t][e], PHI);
          dp[t][2 * e] = prm(dw[2 * t + 1][e], dw[2 * t][e], PLO); dp[t][2 * e + 1] = prm(dw[2 * t + 1][e], dw[2 * t][e], PHI);
        }
#pragma unroll
      for (int t = 0; t < 2; ++t)
#pragma unroll
        for (int e = 0; e < 8; ++e) asm volatile("" : "+v"(gp[t][e]), "+v"(up[t][e]), "+v"(dp[t][e]));
      bar();
      TS(8);
      {
        const v2u hp = *(const v2u*)&hn[32 * w + 2 * r16];
        float ag[8], au[8];
#pragma unroll
        for (int e = 0; e < 8; ++e) {
          ag[e] = dot2(gp[1][e], hp.y, dot2(gp[0][e], hp.x, 0.f));
          au[e] = dot2(up[1][e], hp.y, dot2(up[0][e], hp.x, 0.f));
        }
        float s[8];
#pragma unroll
        for (int k = 0; k < 8; ++k) s[k] = sw32(ag[k], au[k]);
        v4f t;
#pragma unroll
        for (int k = 0; k < 4; ++k) {
          float a = sw16(s[k], s[k + 4]);
          a += dppf<R8>(a);
          a += dppf<R4>(a);
          t[k] = a;
        }
        if ((lane & 12) == 0) *(v4f*)&red[w * 64 + (g >> 1) * 32 + 8 * c4 + 4 * (g & 1)] = t;
      }
      bar();
      TS(9);
      {
        v4f gg = *(const v4f*)&red[c8 * 64 + 4 * r8], uu = *(const v4f*)&red[c8 * 64 + 32 + 4 * r8];
        float hh[4];
#pragma unroll
        for (int k = 0; k < 4; ++k) {
          float a = gg[k], d = uu[k];
          a += dppf<X1>(a); d += dppf<X1>(d);
          a += dppf<X2>(a); d += dppf<X2>(d);
          a += dppf<HM>(a); d += dppf<HM>(d);
          hh[k] = rbf(a) * rbf(d);
        }
        const u32 h0 = pk(hh[0], hh[1]), h1 = pk(hh[2], hh[3]);
        float a[8];
#pragma unroll
        for (int e = 0; e < 8; ++e) a[e] = dot2(dp[1][e], h1, dot2(dp[0][e], h0, 0.f));
        u32 p01, p23;
        colred(a, p01, p23);
        const int cg = 16 * w + 2 * c8 + (lane >> 5);
        if ((lane & 24) == 0) pub(rws, OFF_P + ((fb * 64 + j) * 128 + cg) * 16, p01, t0, p23, t1);
      }
      TS(10);
    }
  }
}

extern "C" void kernel_launch(void* const* d_in, const int* in_sizes, int n_in, void* d_out, int out_size, void* d_ws,
                              size_t ws_size, hipStream_t stream) {
  fused_decode<<<256, 512, 0, stream>>>((const u16*)d_in[0], (const u16*)d_in[1], (const u16*)d_in[2], (const u16*)d_in[3],
                                        (const u16*)d_in[4], (const u16*)d_in[6], (const u16*)d_in[7], (const u16*)d_in[8],
                                        (u16*)d_out, (unsigned char*)d_ws);
}
```

```cpp
#include <hip/hip_runtime.h>
#include <stdint.h>

typedef unsigned short u16;
typedef unsigned u32;
typedef u32 v4u __attribute__((ext_vector_type(4)));
typedef u32 v2u __attribute__((ext_vector_type(2)));
typedef float v4f __attribute__((ext_vector_type(4)));
typedef float v2f __attribute__((ext_vector_type(2)));
typedef __bf16 bf2 __attribute__((ext_vector_type(2)));
typedef __bf16 bf8 __attribute__((ext_vector_type(8)));
typedef __amdgpu_buffer_rsrc_t rsrc_t;
#define DI __device__ __forceinline__
#define SB() __builtin_amdgcn_sched_barrier(0)

constexpr int D = 512, NH = 32, SK = 4096, DH = 128, WIN = 1024, F = 1376;
constexpr int NJ = F / 32;
constexpr u32 OFF_ML = 4096;
constexpr u32 OFF_H = 8192;
constexpr u32 OFF_Q = 16384;
constexpr u32 OFF_Y = OFF_Q + (1u << 20);
constexpr u32 OFF_P = OFF_Y + (1u << 19);
constexpr u32 WS_BYTES = OFF_P + (1u << 19);
constexpr int NT = 2, SC1 = 16;
constexpr int SPIN = 400000;
constexpr u32 PLO = 0x05040100u, PHI = 0x07060302u;
constexpr float LOG2E = 1.4426950408889634f;


DI float f_(u32 u) { return __builtin_bit_cast(float, u); }
DI u32 u_(float f) { return __builtin_bit_cast(u32, f); }
DI float blo(u32 u) { return f_(u << 16); }
DI float bhi(u32 u) { return f_(u & 0xffff0000u); }
DI u32 pk(float a, float b) {
  v2f v = {a, b};
  return __builtin_bit_cast(u32, __builtin_convertvector(v, bf2));
}
DI float rbf(float a) { return f_(pk(a, a) & 0xffff0000u); }
DI float dot2(u32 a, u32 b, float c) {
  return __builtin_amdgcn_fdot2_f32_bf16(__builtin_bit_cast(bf2, a), __builtin_bit_cast(bf2, b), c, false);
}
DI u32 prm(u32 second, u32 first, u32 sel) { return __builtin_amdgcn_perm(second, first, sel); }
template <int C> DI u32 dppu(u32 v) { return (u32)__builtin_amdgcn_mov_dpp((int)v, C, 0xf, 0xf, true); }
template <int C> DI float dppf(float v) { return f_(dppu<C>(u_(v))); }
constexpr int X1 = 0xB1, X2 = 0x4E, HM = 0x141, RM = 0x140, R4 = 0x124, R8 = 0x128;
DI float sw32(float a, float b) {
  auto r = __builtin_amdgcn_permlane32_swap(u_(a), u_(b), false, false);
  return f_(r[0]) + f_(r[1]);
}
DI float sw16(float a, float b) {
  auto r = __builtin_amdgcn_permlane16_swap(u_(a), u_(b), false, false);
  return f_(r[0]) + f_(r[1]);
}
DI float mx32(float a) {
  auto r = __builtin_amdgcn_permlane32_swap(u_(a), u_(a), false, false);
  return fmaxf(f_(r[0]), f_(r[1]));
}
DI float mx16(float a) {
  auto r = __builtin_amdgcn_permlane16_swap(u_(a), u_(a), false, false);
  return fmaxf(f_(r[0]), f_(r[1]));
}
DI float rowsum(float v) {
  v += dppf<X1>(v); v += dppf<X2>(v); v += dppf<HM>(v); v += dppf<RM>(v);
  return v;
}
DI float wavesum(float v) { v = rowsum(v); v = sw16(v, v); return sw32(v, v); }
DI float ex2(float v) { return __builtin_amdgcn_exp2f(v); }
DI float rcp(float v) { return __builtin_amdgcn_rcpf(v); }

DI rsrc_t mkr(const void* p, u32 bytes) { return __builtin_amdgcn_make_buffer_rsrc((void*)p, 0, bytes, 0x00020000); }
template <int AUX> DI v4u ld(rsrc_t r, u32 voff, u32 soff) { return __builtin_amdgcn_raw_buffer_load_b128(r, voff, soff, AUX); }
DI void pub(rsrc_t r, u32 off, u32 a, u32 t0, u32 b, u32 t1) {
  v4u v = {a, t0, b, t1};
  __builtin_amdgcn_raw_buffer_store_b128(v, r, off, 0, SC1);
}
DI void bar() { asm volatile("s_waitcnt lgkmcnt(0)\n\ts_barrier" ::: "memory"); }

template <int N> DI void poll(v4u (&v)[N], rsrc_t r, u32 voff, u32 stride, u32 t0, u32 t1) {
  for (int it = 0; it < SPIN; ++it) {
    asm volatile("" ::: "memory");
#pragma unroll
    for (int n = 0; n < N; ++n) v[n] = ld<SC1>(r, voff, n * stride);
    bool ok = true;
#pragma unroll
    for (int n = 0; n < N; ++n) ok = ok && (v[n].y == t0) && (v[n].w == t1);
    if (__all(ok)) break;
  }
}

DI float ln_rinv(float var) { return rcp(rbf(__builtin_amdgcn_sqrtf(rbf(rbf(var) + 1e-5f)))); }
DI u32 ln_pair(u32 v, float m, float rinv) { return pk(rbf(blo(v) - m) * rinv, rbf(bhi(v) - m) * rinv); }

DI void colred(const float (&a)[8], u32& p01, u32& p23) {
  float s[4];
#pragma unroll
  for (int k = 0; k < 4; ++k) s[k] = sw32(a[k], a[k + 4]);
#pragma unroll
  for (int k = 0; k < 4; ++k) s[k] = sw16(s[k], s[k]);
#pragma unroll
  for (int k = 0; k < 4; ++k) s[k] += dppf<R8>(s[k]);
  p01 = pk(s[0], s[1]);
  p23 = pk(s[2], s[3]);
}
DI float red4(float v0, float v1, float v2, float v3) {
  float s0 = sw32(v0, v2), s1 = sw32(v1, v3);
  float t = sw16(s0, s1);
  return rowsum(t);
}

DI float red4b(float v0, float v1, float v2, float v3) {
  float s0 = sw32(v0, v2), s1 = sw32(v1, v3);
  float t = sw16(s0, s1);
  return t + dppf<R8>(t);
}

__global__ void __launch_bounds__(512, 2)
fused_decode(const u16* __restrict__ x, const u16* __restrict__ kc, const u16* __restrict__ vc, const u16* __restrict__ wq,
             const u16* __restrict__ wo, const u16* __restrict__ wg, const u16* __restrict__ wu,
             const u16* __restrict__ wd, u16* __restrict__ out, unsigned char* __restrict__ ws) {
  __shared__ __attribute__((aligned(16))) u32 qs[64];
  __shared__ __attribute__((aligned(16))) float ml[16];
  __shared__ __attribute__((aligned(16))) float op[8 * 128];
  __shared__ __attribute__((aligned(16))) u32 ob[64];
  __shared__ __attribute__((aligned(16))) u32 hn[256];
  __shared__ __attribute__((aligned(16))) float red[8 * 64];
  const int blk = blockIdx.x, xc = blk & 7, ii = blk >> 3;
  const int h = 4 * xc + (ii & 3), r = ii >> 2, b = r >> 1, half = r & 1;
  const int tid = threadIdx.x, lane = tid & 63, w = __builtin_amdgcn_readfirstlane(tid >> 6);
  const int g = lane >> 4, c = lane & 15, r8 = lane >> 3, c8 = lane & 7;

  asm volatile("" ::"s"(x), "s"(kc), "s"(vc), "s"(wq), "s"(wo), "s"(wg), "s"(wu), "s"(wd), "s"(out), "s"(ws));
  const rsrc_t rws = mkr(ws, WS_BYTES);
  v2u ep = __builtin_amdgcn_raw_buffer_load_b64(rws, 0, 0, SC1);
  u32 ta = 0, tb = 0;
  const size_t kvbase = ((size_t)(b * NH + h) * SK + (SK - WIN) + half * 512) * DH;
  const rsrc_t rk = mkr(kc + kvbase, 512 * DH * 2);
  const rsrc_t rv = mkr(vc + kvbase, 512 * DH * 2);
  const rsrc_t rwo = mkr(wo + (size_t)h * DH * D, DH * D * 2);
  const rsrc_t rx = mkr(x, 4 * D * 2);
  const u32 kvoff = (64 * w + g) * 256 + 16 * c;
  const u32 wooff = 16 * r8 * 1024 + 128 * w + 16 * c8;

  if (w == 0) {
    const rsrc_t rq = mkr(wq + (size_t)64 * r * (NH * DH) + h * DH, 64 * NH * DH * 2 - h * DH * 2);
    const int bb = c >> 2, sub = (c & 3) + 4 * g;
    v4u xs[4], xa[2], q[16];
#pragma unroll
    for (int k = 0; k < 4; ++k) xs[k] = ld<0>(rx, bb * 1024 + 16 * sub, 256 * k);
#pragma unroll
    for (int k = 0; k < 2; ++k) xa[k] = ld<0>(rx, bb * 1024 + 128 * r + 32 * g, 16 * k);
    SB();
#pragma unroll
    for (int i = 0; i < 16; ++i) q[i] = ld<NT>(rq, 16 * g * 8192 + 16 * c, i * 8192);
    SB();
    float s = 0.f;
#pragma unroll
    for (int k = 0; k < 4; ++k)
#pragma unroll
      for (int e = 0; e < 4; ++e) s += blo(xs[k][e]) + bhi(xs[k][e]);
    s += dppf<X1>(s); s += dppf<X2>(s);
    s = sw16(s, s); s = sw32(s, s);
    const float mean = s * (1.f / D);
    float vs = 0.f;
#pragma unroll
    for (int k = 0; k < 4; ++k)
#pragma unroll
      for (int e = 0; e < 4; ++e) {
        float d0 = blo(xs[k][e]) - mean, d1 = bhi(xs[k][e]) - mean;
        vs = fmaf(d0, d0, vs); vs = fmaf(d1, d1, vs);
      }
    vs += dppf<X1>(vs); vs += dppf<X2>(vs);
    vs = sw16(vs, vs); vs = sw32(vs, vs);
    const float mb = rbf(mean), rinv = ln_rinv(vs * (1.f / D));
    v4u an[2];
#pragma unroll
    for (int k = 0; k < 2; ++k)
#pragma unroll
      for (int e = 0; e < 4; ++e) an[k][e] = ln_pair(xa[k][e], mb, rinv);
    float qa[8];
#pragma unroll
    for (int j = 0; j < 8; ++j) {
      v4f acc = {0.f, 0.f, 0.f, 0.f};
#pragma unroll
      for (int k = 0; k < 2; ++k) {
        v4u bq;
#pragma unroll
        for (int t = 0; t < 4; ++t) bq[t] = prm(q[8 * k + 2 * t + 1][j >> 1], q[8 * k + 2 * t][j >> 1], (j & 1) ? PHI : PLO);
        acc = __builtin_amdgcn_mfma_f32_16x16x32_bf16(__builtin_bit_cast(bf8, an[k]), __builtin_bit_cast(bf8, bq), acc, 0, 0, 0);
      }
      qa[j] = acc[0];
    }
    ta = ep.x; tb = ep.y;
    asm volatile("" : "+v"(ta), "+v"(tb));
    ta += 1u; tb += 1u;
    const u32 qo = OFF_Q + (((h * 4 + g) * 8 + r) * 64 + c) * 16;
#pragma unroll
    for (int t = 0; t < 4; ++t) pub(rws, qo + 256 * t, u_(qa[2 * t]), ta, u_(qa[2 * t + 1]), tb);
    SB();
  }

  v4u kk[16], vv[16], ww[16];
#pragma unroll
  for (int i = 0; i < 16; ++i) kk[i] = ld<NT>(rk, kvoff, i * 1024);
  SB();
  if (w == 0) {
    v4u qp[8];
    poll<8>(qp, rws, OFF_Q + ((h * 4 + b) * 8 * 64 + lane) * 16, 1024, ta, tb);
    float q0 = 0.f, q1 = 0.f;
#pragma unroll
    for (int n = 0; n < 8; ++n) { q0 += f_(qp[n].x); q1 += f_(qp[n].z); }
    qs[4 * c + g] = pk(q0, q1);
    SB();
  }
#pragma unroll
  for (int i = 0; i < 4; ++i) { vv[i] = ld<NT>(rv, kvoff, i * 1024); ww[i] = ld<NT>(rwo, wooff, i * 1024); }
  SB();
  u32 t0 = ep.x, t1 = ep.y;
  asm volatile("" : "+v"(t0), "+v"(t1));
  t0 += 1u; t1 += 1u;
  bar();

  float p, mw;
  {
    const v4u qv = *(const v4u*)&qs[4 * c];
    float s[16];
#pragma unroll
    for (int i = 0; i < 16; ++i) {
      float a = dot2(kk[i].x, qv.x, 0.f);
      a = dot2(kk[i].y, qv.y, a);
      a = dot2(kk[i].z, qv.z, a);
      s[i] = dot2(kk[i].w, qv.w, a);
    }
    const bool b3 = c & 8, b2 = c & 4, b1 = c & 2, b0 = c & 1;
#pragma unroll
    for (int i = 0; i < 8; ++i) s[i] = (b3 ? s[i + 8] : s[i]) + dppf<RM>(b3 ? s[i] : s[i + 8]);
#pragma unroll
    for (int i = 0; i < 4; ++i) s[i] = (b2 ? s[i + 4] : s[i]) + dppf<HM>(b2 ? s[i] : s[i + 4]);
#pragma unroll
    for (int i = 0; i < 2; ++i) s[i] = (b1 ? s[i + 2] : s[i]) + dppf<X2>(b1 ? s[i] : s[i + 2]);
    const float raw = (b0 ? s[1] : s[0]) + dppf<X1>(b0 ? s[0] : s[1]);
    const float sc = rbf(rbf(raw) * 0.08838834764831845f);
    float m = sc;
    m = fmaxf(m, dppf<X1>(m)); m = fmaxf(m, dppf<X2>(m)); m = fmaxf(m, dppf<HM>(m)); m = fmaxf(m, dppf<RM>(m));
    m = mx32(mx16(m));
    mw = m;
    p = ex2((sc - m) * LOG2E);
    const float l = wavesum(p);
    if (lane == 0) { v2f t = {m, l}; *(v2f*)&ml[2 * w] = t; }
  }
  bar();
  float fw;
  {
    const v2f t = *(const v2f*)&ml[2 * c8];
    float M = t.x;
    M = fmaxf(M, dppf<X1>(M)); M = fmaxf(M, dppf<X2>(M)); M = fmaxf(M, dppf<HM>(M));
    float L = t.y * ex2((t.x - M) * LOG2E);
    L += dppf<X1>(L); L += dppf<X2>(L); L += dppf<HM>(L);
    fw = ex2((mw - M) * LOG2E) * rcp(L);
    if (w == 0 && lane == 0) pub(rws, OFF_ML + (b * 64 + h * 2 + half) * 16, u_(M), t0, u_(L), t1);
  }
  const u32 ppk = pk(p, dppf<X1>(p));
  SB();

  float acc[8] = {0.f, 0.f, 0.f, 0.f, 0.f, 0.f, 0.f, 0.f};
  u32 wp[8][8];
#define PVSTEP(t)                                                                       \
  do {                                                                                  \
    const u32 pp = dppu<0x150 + 2 * (t)>(ppk);                                          \
    _Pragma("unroll") for (int e = 0; e < 4; ++e) {                                     \
      acc[2 * e] = dot2(prm(vv[2 * (t) + 1][e], vv[2 * (t)][e], PLO), pp, acc[2 * e]);  \
      acc[2 * e + 1] = dot2(prm(vv[2 * (t) + 1][e], vv[2 * (t)][e], PHI), pp, acc[2 * e + 1]); \
      wp[t][2 * e] = prm(ww[2 * (t) + 1][e], ww[2 * (t)][e], PLO);                      \
      wp[t][2 * e + 1] = prm(ww[2 * (t) + 1][e], ww[2 * (t)][e], PHI);                  \
    }                                                                                   \
    _Pragma("unroll") for (int e = 0; e < 8; ++e) asm volatile("" : "+v"(wp[t][e]));    \
    SB();                                                                               \
  } while (0)
#define VWLD(i) do { vv[i] = ld<NT>(rv, kvoff, (i) * 1024); ww[i] = ld<NT>(rwo, wooff, (i) * 1024); } while (0)
  VWLD(4); VWLD(5); VWLD(6); VWLD(7); SB();
  PVSTEP(0);
  VWLD(8); VWLD(9); SB();
  PVSTEP(1);
  VWLD(10); VWLD(11); SB();
  PVSTEP(2);
  VWLD(12); VWLD(13); SB();
  PVSTEP(3);
  VWLD(14); VWLD(15); SB();
  PVSTEP(4); PVSTEP(5); PVSTEP(6); PVSTEP(7);
  {
    float s[4];
#pragma unroll
    for (int k = 0; k < 4; ++k) s[k] = sw32(acc[k], acc[k + 4]);
    v2f o = {sw16(s[0], s[2]) * fw, sw16(s[1], s[3]) * fw};
    *(v2f*)&op[w * 128 + 8 * c + 2 * g] = o;
  }
  bar();
  {
    const v2f t = *(const v2f*)&op[r8 * 128 + 16 * w + 2 * c8];
    float a = t.x + dppf<R8>(t.x), d = t.y + dppf<R8>(t.y);
    a = sw16(a, a); d = sw16(d, d);
    a = sw32(a, a); d = sw32(d, d);
    if (lane < 8) ob[8 * w + lane] = pk(a, d);
  }
  bar();
  {
    const v4u o0 = *(const v4u*)&ob[8 * r8], o1 = *(const v4u*)&ob[8 * r8 + 4];
    float a[8] = {0.f, 0.f, 0.f, 0.f, 0.f, 0.f, 0.f, 0.f};
#pragma unroll
    for (int t = 0; t < 8; ++t)
#pragma unroll
      for (int e = 0; e < 8; ++e) a[e] = dot2(wp[t][e], t < 4 ? o0[t & 3] : o1[t & 3], a[e]);
    u32 p01, p23;
    colred(a, p01, p23);
    const int cg = 16 * w + 2 * c8 + (lane >> 5);
    if ((lane & 24) == 0) pub(rws, OFF_Y + ((b * 64 + h * 2 + half) * 128 + cg) * 16, p01, t0, p23, t1);
  }

  if (ii >= 24) {
    const int ri = xc + 8 * (ii - 24), rb = ri >> 4, cb = ri & 15;
    const u32 xraw = x[rb * D + 32 * cb + (lane & 31)];
    v4u mlv, yv;
    const u32 mlo = OFF_ML + (rb * 64 + 8 * w + r8) * 16;
    const u32 yo = OFF_Y + ((rb * 64 + 8 * w + r8) * 128 + 8 * cb + c8) * 16;
    {
      v4u a1[1];
      poll<1>(a1, rws, mlo, 0, t0, t1);
      mlv = a1[0];
    }
    const float M = f_(mlv.x), L = f_(mlv.z);
    const float Mo = dppf<R8>(M), Lo = dppf<R8>(L);
    const float Ms = fmaxf(M, Mo);
    const float e = L * ex2((M - Ms) * LOG2E), eo = Lo * ex2((Mo - Ms) * LOG2E);
    const float wt = e * rcp(e + eo);
    {
      v4u a1[1];
      poll<1>(a1, rws, yo, 0, t0, t1);
      yv = a1[0];
    }
    const int rslot = (4 * c8 + g) * 8 + w;
    {
      const float y = red4b(blo(yv.x) * wt, bhi(yv.x) * wt, blo(yv.z) * wt, bhi(yv.z) * wt);
      if (!(lane & 8)) red[rslot] = y;
    }
    bar();
    float hv = 0.f;
    if (w == 0) {
      const v4f t = *(const v4f*)&red[(lane & 31) * 8 + 4 * (lane >> 5)];
      const float s = (t[0] + t[1]) + (t[2] + t[3]);
      hv = rbf(blo(xraw) + rbf(sw32(s, s)));
      const u32 hp = prm(u_(dppf<X1>(hv)), u_(hv), PHI);
      const u32 ho = dppu<X2>(hp);
      if ((lane & 35) == 0) pub(rws, OFF_H + (rb * 128 + 8 * cb + (lane >> 2)) * 16, hp, t0, ho, t1);
    }
    if (ri == 0 && w == 7) {
      v4u hv3[1];
      poll<1>(hv3, rws, OFF_H + ((lane & 3) * 128) * 16, 0, t0, t1);
      if (lane == 0) {
        v2u e2 = {t0, t1};
        __builtin_amdgcn_raw_buffer_store_b64(e2, rws, 0, 0, SC1);
      }
    }
    float dn = 0.f;
    if (w < 6) {
      v4u pv[1];
      const int j = 8 * w + r8;
      const bool on = j < NJ;
      poll<1>(pv, rws, OFF_P + ((rb * 64 + (on ? j : NJ - 1)) * 128 + 8 * cb + c8) * 16, 0, t0, t1);
      dn = red4b(on ? blo(pv[0].x) : 0.f, on ? bhi(pv[0].x) : 0.f, on ? blo(pv[0].z) : 0.f, on ? bhi(pv[0].z) : 0.f);
    }
    if (!(lane & 8)) op[rslot] = dn;
    bar();
    if (w == 0) {
      const v4f t = *(const v4f*)&op[(lane & 31) * 8 + 4 * (lane >> 5)];
      const float s = (t[0] + t[1]) + (t[2] + t[3]);
      const float o = hv + rbf(sw32(s, s));
      const u32 po = pk(o, dppf<X1>(o));
      if ((lane & 33) == 0) __builtin_amdgcn_raw_buffer_store_b32(po, mkr(out, 4 * D * 2), (rb * D + 32 * cb + lane) * 2, 0, NT);
    }
  } else {
    const int j = 2 * (xc + 8 * (ii >> 3)) + ((ii >> 2) & 1), fb = ii & 3;
    if (j < NJ) {
      asm volatile("s_barrier" ::: "memory");
      const int r16 = lane >> 2, c4 = lane & 3;
      const rsrc_t rg = mkr(wg + 32 * j, D * F * 2 - 64 * j);
      const rsrc_t ru = mkr(wu + 32 * j, D * F * 2 - 64 * j);
      const rsrc_t rd = mkr(wd + (size_t)32 * j * D, 32 * D * 2);
      const int wr = (w + 2 * fb + (j & 1)) & 7, wc = (w + 2 * fb) & 7;
      const u32 goff = (64 * wr + 4 * r16) * (F * 2) + 16 * c4;
      const u32 doff = 4 * r8 * 1024 + 128 * wc + 16 * c8;
      v4u gw[4], uw[4], dw[4];
#pragma unroll
      for (int i = 0; i < 4; ++i) { gw[i] = ld<0>(rg, goff, i * F * 2); uw[i] = ld<0>(ru, goff, i * F * 2); }
#pragma unroll
      for (int i = 0; i < 4; ++i) dw[i] = ld<0>(rd, doff, i * 1024);
      SB();
      if (w == 0) {
        v4u hv[2];
        poll<2>(hv, rws, OFF_H + (fb * 128 + lane) * 16, 1024, t0, t1);
        float v[8] = {blo(hv[0].x), bhi(hv[0].x), blo(hv[0].z), bhi(hv[0].z), blo(hv[1].x), bhi(hv[1].x), blo(hv[1].z), bhi(hv[1].z)};
        const float mean = wavesum(((v[0] + v[1]) + (v[2] + v[3])) + ((v[4] + v[5]) + (v[6] + v[7]))) * (1.f / D);
        float vs = 0.f;
#pragma unroll
        for (int k = 0; k < 8; ++k) { float d = v[k] - mean; vs = fmaf(d, d, vs); }
        const float mb = rbf(mean), rinv = ln_rinv(wavesum(vs) * (1.f / D));
        v2u a = {ln_pair(hv[0].x, mb, rinv), ln_pair(hv[0].z, mb, rinv)};
        v2u d = {ln_pair(hv[1].x, mb, rinv), ln_pair(hv[1].z, mb, rinv)};
        *(v2u*)&hn[2 * lane] = a;
        *(v2u*)&hn[128 + 2 * lane] = d;
      }
      u32 gp[2][8], up[2][8], dp[2][8];
#pragma unroll
      for (int t = 0; t < 2; ++t)
#pragma unroll
        for (int e = 0; e < 4; ++e) {
          gp[t][2 * e] = prm(gw[2 * t + 1][e], gw[2 * t][e], PLO); gp[t][2 * e + 1] = prm(gw[2 * t + 1][e], gw[2 * t][e], PHI);
          up[t][2 * e] = prm(uw[2 * t + 1][e], uw[2 * t][e], PLO); up[t][2 * e + 1] = prm(uw[2 * t + 1][e], uw[2 * t][e], PHI);
          dp[t][2 * e] = prm(dw[2 * t + 1][e], dw[2 * t][e], PLO); dp[t][2 * e + 1] = prm(dw[2 * t + 1][e], dw[2 * t][e], PHI);
        }
#pragma unroll
      for (int t = 0; t < 2; ++t)
#pragma unroll
        for (int e = 0; e < 8; ++e) asm volatile("" : "+v"(gp[t][e]), "+v"(up[t][e]), "+v"(dp[t][e]));
      bar();
      {
        const v2u hp = *(const v2u*)&hn[32 * wr + 2 * r16];
        float ag[8], au[8];
#pragma unroll
        for (int e = 0; e < 8; ++e) {
          ag[e] = dot2(gp[1][e], hp.y, dot2(gp[0][e], hp.x, 0.f));
          au[e] = dot2(up[1][e], hp.y, dot2(up[0][e], hp.x, 0.f));
        }
        float s[8];
#pragma unroll
        for (int k = 0; k < 8; ++k) s[k] = sw32(ag[k], au[k]);
        v4f t;
#pragma unroll
        for (int k = 0; k < 4; ++k) {
          float a = sw16(s[k], s[k + 4]);
          a += dppf<R8>(a);
          a += dppf<R4>(a);
          t[k] = a;
        }
        if ((lane & 12) == 0) *(v4f*)&red[wr * 64 + (g >> 1) * 32 + 8 * c4 + 4 * (g & 1)] = t;
      }
      bar();
      {
        v4f gg = *(const v4f*)&red[c8 * 64 + 4 * r8], uu = *(const v4f*)&red[c8 * 64 + 32 + 4 * r8];
        float hh[4];
#pragma unroll
        for (int k = 0; k < 4; ++k) {
          float a = gg[k], d = uu[k];
          a += dppf<X1>(a); d += dppf<X1>(d);
          a += dppf<X2>(a); d += dppf<X2>(d);
          a += dppf<HM>(a); d += dppf<HM>(d);
          hh[k] = rbf(a) * rbf(d);
        }
        const u32 h0 = pk(hh[0], hh[1]), h1 = pk(hh[2], hh[3]);
        float a[8];
#pragma unroll
        for (int e = 0; e < 8; ++e) a[e] = dot2(dp[1][e], h1, dot2(dp[0][e], h0, 0.f));
        u32 p01, p23;
        colred(a, p01, p23);
        const int cg = 16 * wc + 2 * c8 + (lane >> 5);
        if ((lane & 24) == 0) pub(rws, OFF_P + ((fb * 64 + j) * 128 + cg) * 16, p01, t0, p23, t1);
      }
    }
  }
}

extern "C" void kernel_launch(void* const* d_in, const int* in_sizes, int n_in, void* d_out, int out_size, void* d_ws,
                              size_t ws_size, hipStream_t stream) {
  fused_decode<<<256, 512, 0, stream>>>((const u16*)d_in[0], (const u16*)d_in[1], (const u16*)d_in[2], (const u16*)d_in[3],
                                        (const u16*)d_in[4], (const u16*)d_in[6], (const u16*)d_in[7], (const u16*)d_in[8],
                                        (u16*)d_out, (unsigned char*)d_ws);
}
```
